```python
import torch

def _softmax(x, axis=-1):
    return torch.softmax(x, dim=axis)
def _gelu(x):
    return torch.nn.functional.gelu(x, approximate="tanh")
def _silu(x):
    return torch.nn.functional.silu(x)
def _sigmoid(x):
    return torch.sigmoid(x)
def _layernorm(x, axis=-1, eps=1e-5):
    m = x.mean(dim=axis, keepdim=True)
    v = x.var(dim=axis, keepdim=True, unbiased=False)
    return (x - m) / torch.sqrt(v + eps)
def _scatter_add(tgt, idx, src, axis=0):
    return tgt.clone().index_add_(axis, idx, src)
def _segment_sum(src, seg, n):
    out = torch.zeros((n,) + src.shape[1:], dtype=src.dtype)
    return out.index_add_(0, seg, src)
def _pad(x, pw):
    flat = tuple(v for pair in reversed(pw) for v in pair)
    return torch.nn.functional.pad(x, flat)

kernel_name = 'interleave_layers__linear_attn_block__dense_activation_gelu__prompt'

def setup_inputs():

    _w = (lambda a: (lambda t: t.to(torch.bfloat16) if t.is_floating_point() else t)(torch.as_tensor(a)))
    def _A_setup():
        B, S, D = 1, 2048, 640
        return {
            "x": _w((lambda *s: torch.randn(*s, dtype=torch.bfloat16) * 0.05)(B, S, D)),
            "wqkv": _w((lambda *s: torch.randn(*s, dtype=torch.bfloat16) * 0.05)(D, 3 * D)),
            "wo": _w((lambda *s: torch.randn(*s, dtype=torch.bfloat16) * 0.05)(D, D)),
        }
    _A_in = _A_setup()

    def _B_setup():
        B, S, D, F = 1, 2048, 640, 1728
        return {
            "x": _w((lambda *s: torch.randn(*s, dtype=torch.bfloat16) * 0.05)(B, S, D)),
            "w1": _w((lambda *s: torch.randn(*s, dtype=torch.bfloat16) * 0.8)(D, F) * (D ** -0.5)),
            "b1": _w((lambda *s: torch.randn(*s, dtype=torch.bfloat16) * 0.8)(F) * 0.01),
            "w2": _w((lambda *s: torch.randn(*s, dtype=torch.bfloat16) * 0.8)(F, D) * (F ** -0.5)),
            "b2": _w((lambda *s: torch.randn(*s, dtype=torch.bfloat16) * 0.8)(D) * 0.01),
        }
    _B_in = _B_setup()

    return {
        "A_x": _A_in["x"],
        "A_wqkv": _A_in["wqkv"],
        "A_wo": _A_in["wo"],
        "B_x": _B_in["x"],
        "B_w1": _B_in["w1"],
        "B_b1": _B_in["b1"],
        "B_w2": _B_in["w2"],
        "B_b2": _B_in["b2"],
    }

def reference(A_x, A_wqkv, A_wo, B_x, B_w1, B_b1, B_w2, B_b2):
    def _A_ref(x, wqkv, wo):
        B, S, D = x.shape
        H = 32; Dh = D // H
        qkv = (lambda x, *d: x.permute(*d))((x @ wqkv).reshape(B, S, 3, H, Dh), 2, 0, 3, 1, 4)
        q, k, v = qkv[0], qkv[1], qkv[2]
        phi = lambda t: torch.where(t > 0, t + 1, torch.exp(t)) + 1e-6
        pq, pk = phi(q), phi(k)
        kv = torch.einsum("bhsd,bhse->bhde", pk, v)
        z = torch.einsum("bhsd->bhd", pk)
        num = torch.einsum("bhqd,bhde->bhqe", pq, kv)
        den = torch.einsum("bhqd,bhd->bhq", pq, z)[..., None] + 1e-6
        o = num / den
        return (lambda x, *d: x.permute(*d))(o, 0, 2, 1, 3).reshape(B, S, D) @ wo

    def _B_ref(x, w1, b1, w2, b2):
        h = torch.einsum("bsd,df->bsf", x, w1) + b1
        h = _gelu(h)
        return torch.einsum("bsf,fd->bsd", h, w2) + b2

    x = A_x
    x = x + _A_ref(_layernorm(x, axis=-1), A_wqkv, A_wo)
    x = x + _B_ref(_layernorm(x, axis=-1), B_w1, B_b1, B_w2, B_b2)
    x = x + _A_ref(_layernorm(x, axis=-1), A_wqkv, A_wo)
    x = x + _B_ref(_layernorm(x, axis=-1), B_w1, B_b1, B_w2, B_b2)
    return x
```

```cpp
#include <hip/hip_runtime.h>
#include <hip/hip_bf16.h>
#include <stdint.h>
#define NLAYERS 2

typedef __attribute__((ext_vector_type(8))) short bf16x8;
typedef __attribute__((ext_vector_type(4))) short s16x4;
typedef __attribute__((ext_vector_type(4))) float f32x4;
typedef __attribute__((ext_vector_type(2))) unsigned u32x2;
typedef __attribute__((ext_vector_type(4))) unsigned u32x4;
typedef unsigned short u16;
#define LDS_AS __attribute__((address_space(3)))

constexpr int S_ = 2048, D_ = 640, F_ = 1728;
constexpr int XS_STRIDE = 1296;
constexpr int XS_BYTES = 64 * XS_STRIDE;

__device__ __forceinline__ float bf2f(u16 v) { return __uint_as_float(((unsigned)v) << 16); }
__device__ __forceinline__ u16 f2bf(float f) {
  unsigned u = __float_as_uint(f);
  u += 0x7FFFu + ((u >> 16) & 1u);
  return (u16)(u >> 16);
}
__device__ __forceinline__ float bfr(float f) { return bf2f(f2bf(f)); }
__device__ __forceinline__ unsigned pack2(float a, float b) { return (unsigned)f2bf(a) | ((unsigned)f2bf(b) << 16); }

#define WAIT_VM(n) asm volatile("s_waitcnt vmcnt(" #n ")" ::: "memory")
#define WAIT_LGKM0 asm volatile("s_waitcnt lgkmcnt(0)" ::: "memory")
#define BAR __builtin_amdgcn_s_barrier()

template <int N> __device__ __forceinline__ void wait_vm() {
  if constexpr (N == 0) WAIT_VM(0);
  else if constexpr (N == 2) WAIT_VM(2);
  else if constexpr (N == 3) WAIT_VM(3);
  else if constexpr (N == 4) WAIT_VM(4);
  else if constexpr (N == 6) WAIT_VM(6);
  else if constexpr (N == 8) WAIT_VM(8);
  else if constexpr (N == 9) WAIT_VM(9);
  else if constexpr (N == 12) WAIT_VM(12);
  else if constexpr (N == 14) WAIT_VM(14);
  else if constexpr (N == 15) WAIT_VM(15);
  else if constexpr (N == 18) WAIT_VM(18);
  else if constexpr (N == 20) WAIT_VM(20);
  else if constexpr (N == 24) WAIT_VM(24);
  else if constexpr (N == 1) WAIT_VM(1);
  else if constexpr (N == 10) WAIT_VM(10);
  else WAIT_VM(0);
}

__device__ __forceinline__ void glds16(const void* g, char* lds_base) {
  unsigned keep;
  const unsigned base = __builtin_amdgcn_readfirstlane((unsigned)(size_t)lds_base);
  asm volatile("s_mov_b32 %0, m0\n\ts_mov_b32 m0, %2\n\ts_nop 0\n\tglobal_load_lds_dwordx4 %1, off\n\ts_mov_b32 m0, %0"
               : "=&s"(keep) : "v"(g), "s"(base) : "memory");
}
__device__ __forceinline__ s16x4 tr_read(const char* p) {
  return __builtin_amdgcn_ds_read_tr16_b64_v4i16((LDS_AS s16x4*)p);
}
__device__ __forceinline__ bf16x8 cat8(s16x4 a, s16x4 b) {
  bf16x8 r;
  r[0] = a[0]; r[1] = a[1]; r[2] = a[2]; r[3] = a[3];
  r[4] = b[0]; r[5] = b[1]; r[6] = b[2]; r[7] = b[3];
  return r;
}

__device__ __forceinline__ void ln_tile(const u16* __restrict__ x, int row0, char* xs, int wave, int lane) {
  const int j = lane & 15, rsub = lane >> 4;
#pragma unroll
  for (int pass = 0; pass < 4; ++pass) {
    const int r = wave * 16 + pass * 4 + rsub;
    const u16* src = x + (size_t)(row0 + r) * D_;
    float v[40];
#pragma unroll
    for (int c = 0; c < 5; ++c) {
      u32x4 q = *(const u32x4*)(src + (j + 16 * c) * 8);
#pragma unroll
      for (int e = 0; e < 4; ++e) {
        v[c * 8 + 2 * e] = __uint_as_float(q[e] << 16);
        v[c * 8 + 2 * e + 1] = __uint_as_float(q[e] & 0xFFFF0000u);
      }
    }
    float s = 0.f;
#pragma unroll
    for (int e = 0; e < 40; ++e) s += v[e];
    s += __shfl_xor(s, 1); s += __shfl_xor(s, 2); s += __shfl_xor(s, 4); s += __shfl_xor(s, 8);
    const float mean = s * (1.f / 640.f);
    float q2 = 0.f;
#pragma unroll
    for (int e = 0; e < 40; ++e) { float d = v[e] - mean; q2 += d * d; }
    q2 += __shfl_xor(q2, 1); q2 += __shfl_xor(q2, 2); q2 += __shfl_xor(q2, 4); q2 += __shfl_xor(q2, 8);
    const float rstd = 1.0f / sqrtf(q2 * (1.f / 640.f) + 1e-5f);
#pragma unroll
    for (int c = 0; c < 5; ++c) {
      u32x4 o;
#pragma unroll
      for (int e = 0; e < 4; ++e)
        o[e] = pack2((v[c * 8 + 2 * e] - mean) * rstd, (v[c * 8 + 2 * e + 1] - mean) * rstd);
      *(u32x4*)(xs + r * XS_STRIDE + (j + 16 * c) * 16) = o;
    }
  }
}

constexpr int K1_STG = 32 * 480;
constexpr int K1_NB = 4;
__global__ __launch_bounds__(256, 1) void k1_qkv(const u16* __restrict__ x, const u16* __restrict__ wqkv,
                                                 u16* __restrict__ pqp, float* __restrict__ part,
                                                 u16* __restrict__ kvf, unsigned* __restrict__ cnt) {
  __shared__ __attribute__((aligned(16))) char smem[XS_BYTES + K1_NB * K1_STG];
  char* xs = smem;
  char* ring = smem + XS_BYTES;
  const int tid = threadIdx.x, lane = tid & 63, wave = __builtin_amdgcn_readfirstlane(tid >> 6);
  const int hg = blockIdx.x & 7, rt = blockIdx.x >> 3;
  const int row0 = rt * 64;
  const int g = lane >> 4, q4 = (lane & 15) >> 2, p4 = lane & 3, l15 = lane & 15;

  auto issue = [&](int s) {
    char* dst = ring + (s % K1_NB) * K1_STG;
#pragma unroll
    for (int i = 0; i < 4; ++i) {
      const int j = i * 4 + wave;
      if (j < 15) {
        const int ci = j * 64 + lane;
        const int r = ci / 30, c = ci % 30;
        const int col = (c / 10) * 640 + hg * 80 + (c % 10) * 8;
        glds16(wqkv + (size_t)(s * 32 + r) * 1920 + col, dst + j * 1024);
      }
    }
  };
  issue(0); issue(1); issue(2);
  ln_tile(x, row0, xs, wave, lane);

  const int wm = wave & 1, wn = wave >> 1;
  const int u0 = wn * 8;
  f32x4 acc[2][8];
#pragma unroll
  for (int i = 0; i < 2; ++i)
#pragma unroll
    for (int u = 0; u < 8; ++u) acc[i][u] = (f32x4){0.f, 0.f, 0.f, 0.f};

  for (int s = 0; s < 20; ++s) {
    if (s + 2 < 20) { if (wave < 3) wait_vm<8>(); else wait_vm<6>(); } else wait_vm<0>();
    WAIT_LGKM0;
    BAR;
    if (s + 3 < 20) issue(s + 3);
    const char* st = ring + (s % K1_NB) * K1_STG;
    bf16x8 xf[2];
#pragma unroll
    for (int i = 0; i < 2; ++i)
      xf[i] = *(const bf16x8*)(xs + (32 * wm + 16 * i + l15) * XS_STRIDE + (32 * s + 8 * g) * 2);
#pragma unroll
    for (int u = 0; u < 8; ++u) {
      if (u0 + u < 15) {
        const char* b = st + (8 * g + q4) * 480 + (u0 + u) * 32 + p4 * 8;
        bf16x8 wf = cat8(tr_read(b), tr_read(b + 4 * 480));
#pragma unroll
        for (int i = 0; i < 2; ++i)
          acc[i][u] = __builtin_amdgcn_mfma_f32_16x16x32_bf16(wf, xf[i], acc[i][u], 0, 0, 0);
      }
    }
  }
  WAIT_LGKM0;
  BAR;

  char* pkimg = ring;
  char* vimg = ring + 64 * 192;
#pragma unroll
  for (int u = 0; u < 8; ++u) {
    const int uu = u0 + u;
    if (uu < 15) {
      const int seg = uu / 5;
      const int n0 = (uu % 5) * 16 + 4 * g;
#pragma unroll
      for (int i = 0; i < 2; ++i) {
        const int m = 32 * wm + 16 * i + l15;
        float vals[4];
#pragma unroll
        for (int r = 0; r < 4; ++r) {
          float t = bfr(acc[i][u][r]);
          if (seg < 2) { t = (t > 0.f ? bfr(t + 1.f) : bfr(__expf(t))) + 1e-6f; }
          vals[r] = t;
        }
        u32x2 pk = {pack2(vals[0], vals[1]), pack2(vals[2], vals[3])};
        const int hh = n0 / 20, d0 = n0 % 20;
        if (seg == 0) {
          *(u32x2*)(pqp + ((size_t)(row0 + m) * 32 + hg * 4 + hh) * 32 + d0) = pk;
        } else if (seg == 1) {
          *(u32x2*)(pkimg + m * 192 + n0 * 2) = pk;
        } else {
          *(u32x2*)(vimg + m * 272 + (hh * 32 + d0) * 2) = pk;
        }
      }
    }
  }
  {
    const int r = tid >> 2, hh = tid & 3;
    u32x2 z2 = {0u, 0u};
    u16* dst = pqp + ((size_t)(row0 + r) * 32 + hg * 4 + hh) * 32 + 20;
    *(u32x2*)(dst) = z2; *(u32x2*)(dst + 4) = z2; *(u32x2*)(dst + 8) = z2;
    u32x2 one = {0x3F80u, 0u};
    *(u32x2*)(vimg + r * 272 + (hh * 32 + 20) * 2) = one;
  }
  __syncthreads();
  {
    const int hh = wave;
    f32x4 kv[2][2];
#pragma unroll
    for (int a = 0; a < 2; ++a)
#pragma unroll
      for (int b = 0; b < 2; ++b) kv[a][b] = (f32x4){0.f, 0.f, 0.f, 0.f};
#pragma unroll
    for (int ks = 0; ks < 2; ++ks) {
      bf16x8 af[2], bfv[2];
#pragma unroll
      for (int dt = 0; dt < 2; ++dt) {
        const char* a = pkimg + (32 * ks + 8 * g + q4) * 192 + (hh * 20 + 16 * dt + 4 * p4) * 2;
        af[dt] = cat8(tr_read(a), tr_read(a + 4 * 192));
      }
#pragma unroll
      for (int et = 0; et < 2; ++et) {
        const char* b = vimg + (32 * ks + 8 * g + q4) * 272 + (hh * 32 + 16 * et + 4 * p4) * 2;
        bfv[et] = cat8(tr_read(b), tr_read(b + 4 * 272));
      }
#pragma unroll
      for (int dt = 0; dt < 2; ++dt)
#pragma unroll
        for (int et = 0; et < 2; ++et)
          kv[dt][et] = __builtin_amdgcn_mfma_f32_16x16x32_bf16(af[dt], bfv[et], kv[dt][et], 0, 0, 0);
    }
    float* pp = part + ((size_t)(hg * 32 + rt) * 4 + hh) * 420;
#pragma unroll
    for (int dt = 0; dt < 2; ++dt)
#pragma unroll
      for (int et = 0; et < 2; ++et) {
        const int e = 16 * et + l15, d0 = 16 * dt + 4 * g;
        if (e < 21 && d0 < 20) *(f32x4*)(pp + e * 20 + d0) = kv[dt][et];
      }
  }
  asm volatile("s_waitcnt vmcnt(0)" ::: "memory");
  __syncthreads();
  unsigned* flag = (unsigned*)xs;
  if (tid == 0) {
    __builtin_amdgcn_fence(__ATOMIC_RELEASE, "agent");
    asm volatile("s_waitcnt vmcnt(0)" ::: "memory");
    unsigned old = __hip_atomic_fetch_add(cnt + hg, 1u, __ATOMIC_RELAXED, __HIP_MEMORY_SCOPE_AGENT);
    unsigned last = (old == 31u) ? 1u : 0u;
    if (last) {
      __builtin_amdgcn_fence(__ATOMIC_ACQUIRE, "agent");
      asm volatile("s_waitcnt vmcnt(0)" ::: "memory");
    }
    *flag = last;
  }
  __syncthreads();
  if (*flag) {
    const float* pb = part + (size_t)(hg * 32) * 4 * 420;
    for (int o = tid; o < 4 * 1024; o += 256) {
      const int hh = o >> 10, e = (o >> 5) & 31, d = o & 31;
      float sum = 0.f;
      if (e < 21 && d < 20) {
        const float* src = pb + hh * 420 + e * 20 + d;
#pragma unroll 8
        for (int r = 0; r < 32; ++r) sum += src[(size_t)r * 4 * 420];
      }
      kvf[(size_t)(hg * 4 + hh) * 1024 + e * 32 + d] = f2bf(sum);
    }
  }
}

constexpr int K2_STG = 32 * 160;
constexpr int K2_NB = 8;
__global__ __launch_bounds__(256, 1) void k2_attn_out(const u16* __restrict__ x, const u16* __restrict__ wo,
                                                      const u16* __restrict__ pqp, const u16* __restrict__ kvf,
                                                      u16* __restrict__ x1) {
  __shared__ __attribute__((aligned(16))) char smem[XS_BYTES + K2_NB * K2_STG];
  char* xs = smem;
  char* ring = smem + XS_BYTES;
  const int tid = threadIdx.x, lane = tid & 63, wave = __builtin_amdgcn_readfirstlane(tid >> 6);
  const int ct = blockIdx.x & 7, rt = blockIdx.x >> 3;
  const int row0 = rt * 64;
  const int g = lane >> 4, q4 = (lane & 15) >> 2, p4 = lane & 3, l15 = lane & 15;

  auto issue = [&](int s) {
    char* dst = ring + (s % K2_NB) * K2_STG;
#pragma unroll
    for (int i = 0; i < 2; ++i) {
      const int j = i * 4 + wave;
      if (j < 5) {
        const int ci = j * 64 + lane;
        const int r = ci / 10, c = ci % 10;
        glds16(wo + (size_t)(s * 32 + r) * 640 + ct * 80 + c * 8, dst + j * 1024);
      }
    }
  };
#pragma unroll
  for (int s = 0; s < K2_NB - 1; ++s) issue(s);

  for (int hi = 0; hi < 8; ++hi) {
    const int h = wave * 8 + hi;
    bf16x8 kf[2];
#pragma unroll
    for (int et = 0; et < 2; ++et) kf[et] = *(const bf16x8*)(kvf + (size_t)h * 1024 + (16 * et + l15) * 32 + 8 * g);
#pragma unroll
    for (int i = 0; i < 4; ++i) {
      const int m = 16 * i + l15;
      bf16x8 pf = *(const bf16x8*)(pqp + ((size_t)(row0 + m) * 32 + h) * 32 + 8 * g);
      f32x4 a0 = {0.f, 0.f, 0.f, 0.f}, a1 = {0.f, 0.f, 0.f, 0.f};
      a0 = __builtin_amdgcn_mfma_f32_16x16x32_bf16(kf[0], pf, a0, 0, 0, 0);
      a1 = __builtin_amdgcn_mfma_f32_16x16x32_bf16(kf[1], pf, a1, 0, 0, 0);
      const float den = bfr(__shfl(a1[0], 16 + l15)) + 1e-6f;
      const float inv = 1.0f / den;
      u32x2 o0 = {pack2(bfr(a0[0]) * inv, bfr(a0[1]) * inv), pack2(bfr(a0[2]) * inv, bfr(a0[3]) * inv)};
      *(u32x2*)(xs + m * XS_STRIDE + (h * 20 + 4 * g) * 2) = o0;
      if (g == 0) {
        u32x2 o1 = {pack2(bfr(a1[0]) * inv, bfr(a1[1]) * inv), pack2(bfr(a1[2]) * inv, bfr(a1[3]) * inv)};
        *(u32x2*)(xs + m * XS_STRIDE + (h * 20 + 16) * 2) = o1;
      }
    }
  }

  f32x4 acc[5];
#pragma unroll
  for (int u = 0; u < 5; ++u) acc[u] = (f32x4){0.f, 0.f, 0.f, 0.f};
  for (int s = 0; s < 20; ++s) {
    if (s + K2_NB - 2 < 20) { if (wave == 0) wait_vm<12>(); else wait_vm<6>(); } else wait_vm<0>();
    WAIT_LGKM0;
    BAR;
    if (s + K2_NB - 1 < 20) issue(s + K2_NB - 1);
    const char* st = ring + (s % K2_NB) * K2_STG;
    bf16x8 xf = *(const bf16x8*)(xs + (16 * wave + l15) * XS_STRIDE + (32 * s + 8 * g) * 2);
#pragma unroll
    for (int u = 0; u < 5; ++u) {
      const char* b = st + (8 * g + q4) * 160 + u * 32 + p4 * 8;
      bf16x8 wf = cat8(tr_read(b), tr_read(b + 4 * 160));
      acc[u] = __builtin_amdgcn_mfma_f32_16x16x32_bf16(wf, xf, acc[u], 0, 0, 0);
    }
  }
  const int m = row0 + 16 * wave + l15;
#pragma unroll
  for (int u = 0; u < 5; ++u) {
    const int n0 = ct * 80 + u * 16 + 4 * g;
    u32x2 xr = *(const u32x2*)(x + (size_t)m * 640 + n0);
    float r0 = __uint_as_float(xr[0] << 16) + bfr(acc[u][0]);
    float r1 = __uint_as_float(xr[0] & 0xFFFF0000u) + bfr(acc[u][1]);
    float r2 = __uint_as_float(xr[1] << 16) + bfr(acc[u][2]);
    float r3 = __uint_as_float(xr[1] & 0xFFFF0000u) + bfr(acc[u][3]);
    u32x2 o = {pack2(r0, r1), pack2(r2, r3)};
    *(u32x2*)(x1 + (size_t)m * 640 + n0) = o;
  }
}

constexpr int K3_STG = 32 * 448;
constexpr int K3_NB = 4;
__global__ __launch_bounds__(256, 1) void k3_mlp_up(const u16* __restrict__ x1, const u16* __restrict__ w1,
                                                    const u16* __restrict__ b1, u16* __restrict__ hbuf) {
  __shared__ __attribute__((aligned(16))) char smem[XS_BYTES + K3_NB * K3_STG];
  char* xs = smem;
  char* ring = smem + XS_BYTES;
  const int tid = threadIdx.x, lane = tid & 63, wave = __builtin_amdgcn_readfirstlane(tid >> 6);
  const int ct = blockIdx.x & 7, rt = blockIdx.x >> 3;
  const int row0 = rt * 64;
  const int g = lane >> 4, q4 = (lane & 15) >> 2, p4 = lane & 3, l15 = lane & 15;

  auto issue = [&](int s) {
    char* dst = ring + (s % K3_NB) * K3_STG;
#pragma unroll
    for (int i = 0; i < 4; ++i) {
      const int j = i * 4 + wave;
      if (j < 14) {
        const int ci = j * 64 + lane;
        const int r = ci / 28, c = ci % 28;
        int col = ct * 224 + c * 8;
        col = col < F_ - 8 ? col : F_ - 8;
        glds16(w1 + (size_t)(s * 32 + r) * F_ + col, dst + j * 1024);
      }
    }
  };
  issue(0); issue(1); issue(2);
  ln_tile(x1, row0, xs, wave, lane);

  const int wm = wave & 1, wn = wave >> 1;
  const int u0 = wn * 7;
  f32x4 acc[2][7];
#pragma unroll
  for (int i = 0; i < 2; ++i)
#pragma unroll
    for (int u = 0; u < 7; ++u) acc[i][u] = (f32x4){0.f, 0.f, 0.f, 0.f};

  for (int s = 0; s < 20; ++s) {
    if (s + 2 < 20) { if (wave < 2) wait_vm<8>(); else wait_vm<6>(); } else wait_vm<0>();
    WAIT_LGKM0;
    BAR;
    if (s + 3 < 20) issue(s + 3);
    const char* st = ring + (s % K3_NB) * K3_STG;
    bf16x8 xf[2];
#pragma unroll
    for (int i = 0; i < 2; ++i)
      xf[i] = *(const bf16x8*)(xs + (32 * wm + 16 * i + l15) * XS_STRIDE + (32 * s + 8 * g) * 2);
#pragma unroll
    for (int u = 0; u < 7; ++u) {
      const char* b = st + (8 * g + q4) * 448 + (u0 + u) * 32 + p4 * 8;
      bf16x8 wf = cat8(tr_read(b), tr_read(b + 4 * 448));
#pragma unroll
      for (int i = 0; i < 2; ++i)
        acc[i][u] = __builtin_amdgcn_mfma_f32_16x16x32_bf16(wf, xf[i], acc[i][u], 0, 0, 0);
    }
  }
#pragma unroll
  for (int u = 0; u < 7; ++u) {
    const int n0 = ct * 224 + (u0 + u) * 16 + 4 * g;
    if (n0 < F_) {
      u32x2 bb = *(const u32x2*)(b1 + n0);
      float bv[4] = {__uint_as_float(bb[0] << 16), __uint_as_float(bb[0] & 0xFFFF0000u),
                     __uint_as_float(bb[1] << 16), __uint_as_float(bb[1] & 0xFFFF0000u)};
#pragma unroll
      for (int i = 0; i < 2; ++i) {
        const int m = row0 + 32 * wm + 16 * i + l15;
        float o[4];
#pragma unroll
        for (int r = 0; r < 4; ++r) {
          float t = bfr(bfr(acc[i][u][r]) + bv[r]);
          float inner = 0.7978845608028654f * (t + 0.044715f * t * t * t);
          o[r] = 0.5f * t * (1.0f + tanhf(inner));
        }
        u32x2 ov = {pack2(o[0], o[1]), pack2(o[2], o[3])};
        *(u32x2*)(hbuf + (size_t)m * F_ + n0) = ov;
      }
    }
  }
}

constexpr int K4_A = 64 * 64;
constexpr int K4_B = 32 * 160;
constexpr int K4_STG = K4_A + K4_B;
constexpr int K4_NB = 8;
__global__ __launch_bounds__(256, 1) void k4_mlp_down(const u16* __restrict__ hbuf, const u16* __restrict__ w2,
                                                      const u16* __restrict__ b2, const u16* __restrict__ x1,
                                                      u16* __restrict__ xo) {
  __shared__ __attribute__((aligned(16))) char smem[K4_NB * K4_STG];
  const int tid = threadIdx.x, lane = tid & 63, wave = __builtin_amdgcn_readfirstlane(tid >> 6);
  const int ct = blockIdx.x & 7, rt = blockIdx.x >> 3;
  const int row0 = rt * 64;
  const int g = lane >> 4, q4 = (lane & 15) >> 2, p4 = lane & 3, l15 = lane & 15;

  auto issue = [&](int s) {
    char* base = smem + (s % K4_NB) * K4_STG;
    {
      const int r = lane >> 2, c = (lane & 3) ^ ((r >> 2) & 3);
      glds16(hbuf + (size_t)(row0 + 16 * wave + r) * F_ + s * 32 + c * 8, base + (16 * wave) * 64);
    }
    char* dst = base + K4_A;
#pragma unroll
    for (int i = 0; i < 2; ++i) {
      const int j = i * 4 + wave;
      if (j < 5) {
        const int ci = j * 64 + lane;
        const int r = ci / 10, c = ci % 10;
        glds16(w2 + (size_t)(s * 32 + r) * 640 + ct * 80 + c * 8, dst + j * 1024);
      }
    }
  };
#pragma unroll
  for (int s = 0; s < K4_NB - 1; ++s) issue(s);

  f32x4 acc[5];
#pragma unroll
  for (int u = 0; u < 5; ++u) acc[u] = (f32x4){0.f, 0.f, 0.f, 0.f};
  for (int s = 0; s < 54; ++s) {
    if (s + K4_NB - 2 < 54) { if (wave == 0) wait_vm<18>(); else wait_vm<12>(); } else wait_vm<0>();
    WAIT_LGKM0;
    BAR;
    if (s + K4_NB - 1 < 54) issue(s + K4_NB - 1);
    const char* st = smem + (s % K4_NB) * K4_STG;
    bf16x8 xf = *(const bf16x8*)(st + (16 * wave + l15) * 64 + ((g ^ ((l15 >> 2) & 3)) * 16));
#pragma unroll
    for (int u = 0; u < 5; ++u) {
      const char* b = st + K4_A + (8 * g + q4) * 160 + u * 32 + p4 * 8;
      bf16x8 wf = cat8(tr_read(b), tr_read(b + 4 * 160));
      acc[u] = __builtin_amdgcn_mfma_f32_16x16x32_bf16(wf, xf, acc[u], 0, 0, 0);
    }
  }
  const int m = row0 + 16 * wave + l15;
#pragma unroll
  for (int u = 0; u < 5; ++u) {
    const int n0 = ct * 80 + u * 16 + 4 * g;
    u32x2 xr = *(const u32x2*)(x1 + (size_t)m * 640 + n0);
    u32x2 bb = *(const u32x2*)(b2 + n0);
    float r0 = __uint_as_float(xr[0] << 16) + bfr(bfr(acc[u][0]) + __uint_as_float(bb[0] << 16));
    float r1 = __uint_as_float(xr[0] & 0xFFFF0000u) + bfr(bfr(acc[u][1]) + __uint_as_float(bb[0] & 0xFFFF0000u));
    float r2 = __uint_as_float(xr[1] << 16) + bfr(bfr(acc[u][2]) + __uint_as_float(bb[1] << 16));
    float r3 = __uint_as_float(xr[1] & 0xFFFF0000u) + bfr(bfr(acc[u][3]) + __uint_as_float(bb[1] & 0xFFFF0000u));
    u32x2 o = {pack2(r0, r1), pack2(r2, r3)};
    *(u32x2*)(xo + (size_t)m * 640 + n0) = o;
  }
}

extern "C" void kernel_launch(void* const* d_in, const int* in_sizes, int n_in,
                              void* d_out, int out_size, void* d_ws, size_t ws_size,
                              hipStream_t stream) {
  const u16* A_x = (const u16*)d_in[0];
  const u16* wqkv = (const u16*)d_in[1];
  const u16* wo = (const u16*)d_in[2];
  const u16* w1 = (const u16*)d_in[4];
  const u16* b1 = (const u16*)d_in[5];
  const u16* w2 = (const u16*)d_in[6];
  const u16* b2 = (const u16*)d_in[7];
  char* ws = (char*)d_ws;
  unsigned* cnt = (unsigned*)ws;
  u16* pqp = (u16*)(ws + 4096);
  float* part = (float*)(ws + 4096 + 4194304);
  u16* kvf = (u16*)(ws + 4096 + 4194304 + 1720320);
  u16* x1 = (u16*)(ws + 4096 + 4194304 + 1720320 + 65536);
  u16* x2 = x1 + (size_t)S_ * D_;
  u16* hbuf = x2 + (size_t)S_ * D_;
  hipMemsetAsync(cnt, 0, 64, stream);
  const u16* xin = A_x;
  for (int layer = 0; layer < NLAYERS; ++layer) {
    u16* xout = layer == 0 ? x2 : (u16*)d_out;
    k1_qkv<<<256, 256, 0, stream>>>(xin, wqkv, pqp, part, kvf, cnt + 8 * layer);
    k2_attn_out<<<256, 256, 0, stream>>>(xin, wo, pqp, kvf, x1);
    k3_mlp_up<<<256, 256, 0, stream>>>(x1, w1, b1, hbuf);
    k4_mlp_down<<<256, 256, 0, stream>>>(hbuf, w2, b2, x1, xout);
    xin = x2;
  }
}
```

```cpp
#include <hip/hip_runtime.h>
#include <hip/hip_bf16.h>
#include <stdint.h>
#define NLAYERS 2

typedef __attribute__((ext_vector_type(8))) short bf16x8;
typedef __attribute__((ext_vector_type(4))) short s16x4;
typedef __attribute__((ext_vector_type(4))) float f32x4;
typedef __attribute__((ext_vector_type(2))) unsigned u32x2;
typedef __attribute__((ext_vector_type(4))) unsigned u32x4;
typedef unsigned short u16;
#define LDS_AS __attribute__((address_space(3)))

constexpr int S_ = 2048, D_ = 640, F_ = 1728;
constexpr int XS_STRIDE = 1296;
constexpr int XS_BYTES = 64 * XS_STRIDE;

__device__ __forceinline__ float bf2f(u16 v) { return __uint_as_float(((unsigned)v) << 16); }
__device__ __forceinline__ u16 f2bf(float f) {
  unsigned u = __float_as_uint(f);
  u += 0x7FFFu + ((u >> 16) & 1u);
  return (u16)(u >> 16);
}
__device__ __forceinline__ unsigned pack2(float a, float b) {
  unsigned r;
  asm("v_cvt_pk_bf16_f32 %0, %1, %2" : "=v"(r) : "v"(a), "v"(b));
  return r;
}
__device__ __forceinline__ float bfr(float f) { return __uint_as_float(pack2(f, f) & 0xFFFF0000u); }

#define WAIT_VM(n) asm volatile("s_waitcnt vmcnt(" #n ")" ::: "memory")
#define WAIT_LGKM0 asm volatile("s_waitcnt lgkmcnt(0)" ::: "memory")
#define BAR __builtin_amdgcn_s_barrier()
#define STAMP(k, i)

template <int N> __device__ __forceinline__ void wait_vm() {
  if constexpr (N == 0) WAIT_VM(0);
  else if constexpr (N == 2) WAIT_VM(2);
  else if constexpr (N == 3) WAIT_VM(3);
  else if constexpr (N == 4) WAIT_VM(4);
  else if constexpr (N == 6) WAIT_VM(6);
  else if constexpr (N == 8) WAIT_VM(8);
  else if constexpr (N == 9) WAIT_VM(9);
  else if constexpr (N == 12) WAIT_VM(12);
  else if constexpr (N == 14) WAIT_VM(14);
  else if constexpr (N == 15) WAIT_VM(15);
  else if constexpr (N == 18) WAIT_VM(18);
  else if constexpr (N == 20) WAIT_VM(20);
  else if constexpr (N == 24) WAIT_VM(24);
  else if constexpr (N == 1) WAIT_VM(1);
  else if constexpr (N == 13) WAIT_VM(13);
  else if constexpr (N == 16) WAIT_VM(16);
  else if constexpr (N == 10) WAIT_VM(10);
  else WAIT_VM(0);
}

__device__ __forceinline__ void glds16(const void* g, char* lds_base) {
  unsigned keep;
  const unsigned base = __builtin_amdgcn_readfirstlane((unsigned)(size_t)lds_base);
  asm volatile("s_mov_b32 %0, m0\n\ts_mov_b32 m0, %2\n\ts_nop 0\n\tglobal_load_lds_dwordx4 %1, off\n\ts_mov_b32 m0, %0"
               : "=&s"(keep) : "v"(g), "s"(base) : "memory");
}
__device__ __forceinline__ s16x4 tr_read(const char* p) {
  return __builtin_amdgcn_ds_read_tr16_b64_v4i16((LDS_AS s16x4*)p);
}
__device__ __forceinline__ bf16x8 cat8(s16x4 a, s16x4 b) {
  bf16x8 r;
  r[0] = a[0]; r[1] = a[1]; r[2] = a[2]; r[3] = a[3];
  r[4] = b[0]; r[5] = b[1]; r[6] = b[2]; r[7] = b[3];
  return r;
}

__device__ __forceinline__ void ln_tile(const u16* __restrict__ x, int row0, char* xs, int wave, int lane) {
  const int j = lane & 15, rsub = lane >> 4;
  u32x4 q[2][5];
#pragma unroll
  for (int pass = 0; pass < 2; ++pass) {
    const u16* src = x + (size_t)(row0 + wave * 8 + pass * 4 + rsub) * D_;
#pragma unroll
    for (int c = 0; c < 5; ++c) q[pass][c] = *(const u32x4*)(src + (j + 16 * c) * 8);
  }
#pragma unroll
  for (int pass = 0; pass < 2; ++pass) {
    const int r = wave * 8 + pass * 4 + rsub;
    float v[40];
#pragma unroll
    for (int c = 0; c < 5; ++c) {
#pragma unroll
      for (int e = 0; e < 4; ++e) {
        v[c * 8 + 2 * e] = __uint_as_float(q[pass][c][e] << 16);
        v[c * 8 + 2 * e + 1] = __uint_as_float(q[pass][c][e] & 0xFFFF0000u);
      }
    }
    float s = 0.f;
#pragma unroll
    for (int e = 0; e < 40; ++e) s += v[e];
    s += __shfl_xor(s, 1); s += __shfl_xor(s, 2); s += __shfl_xor(s, 4); s += __shfl_xor(s, 8);
    const float mean = s * (1.f / 640.f);
    float q2 = 0.f;
#pragma unroll
    for (int e = 0; e < 40; ++e) { float d = v[e] - mean; q2 += d * d; }
    q2 += __shfl_xor(q2, 1); q2 += __shfl_xor(q2, 2); q2 += __shfl_xor(q2, 4); q2 += __shfl_xor(q2, 8);
    const float rstd = __frsqrt_rn(q2 * (1.f / 640.f) + 1e-5f);
    const float nmr = -mean * rstd;
#pragma unroll
    for (int c = 0; c < 5; ++c) {
      u32x4 o;
#pragma unroll
      for (int e = 0; e < 4; ++e)
        o[e] = pack2(fmaf(v[c * 8 + 2 * e], rstd, nmr), fmaf(v[c * 8 + 2 * e + 1], rstd, nmr));
      *(u32x4*)(xs + r * XS_STRIDE + (j + 16 * c) * 16) = o;
    }
  }
}

constexpr int K1_STG = 32 * 480;
constexpr int K1_NB = 5;
__global__ __launch_bounds__(512, 1) void k1_qkv(const u16* __restrict__ x, const u16* __restrict__ wqkv,
                                                 u16* __restrict__ pqp, float* __restrict__ part,
                                                 u16* __restrict__ kvf, unsigned* __restrict__ cnt) {
  __shared__ __attribute__((aligned(16))) char smem[XS_BYTES + K1_NB * K1_STG];
  char* xs = smem;
  char* ring = smem + XS_BYTES;
  const int tid = threadIdx.x, lane = tid & 63, wave = __builtin_amdgcn_readfirstlane(tid >> 6);
  const int hg = (blockIdx.x >> 3) & 7, rt = (blockIdx.x & 7) * 4 + (blockIdx.x >> 6);
  const int row0 = rt * 64;
  const int g = lane >> 4, q4 = (lane & 15) >> 2, p4 = lane & 3, l15 = lane & 15;

  auto issue = [&](int s) {
    char* dst = ring + (s % K1_NB) * K1_STG;
#pragma unroll
    for (int i = 0; i < 2; ++i) {
      const int j = i * 8 + wave;
      if (j < 15) {
        const int ci = j * 64 + lane;
        const int r = ci / 30, c = ci % 30;
        const int col = (c / 10) * 640 + hg * 80 + (c % 10) * 8;
        glds16(wqkv + (size_t)(s * 32 + r) * 1920 + col, dst + j * 1024);
      }
    }
  };
  STAMP(0, 0);
  issue(0); issue(1); issue(2); issue(3);
  ln_tile(x, row0, xs, wave, lane);
  STAMP(0, 1);

  const int wm = wave & 1, wn = wave >> 1;
  const int u0 = wn * 4;
  f32x4 acc[2][4];
#pragma unroll
  for (int i = 0; i < 2; ++i)
#pragma unroll
    for (int u = 0; u < 4; ++u) acc[i][u] = (f32x4){0.f, 0.f, 0.f, 0.f};

  for (int s = 0; s < 20; ++s) {
    if (s + 3 < 20) { if (wave < 7) wait_vm<6>(); else wait_vm<3>(); } else wait_vm<0>();
    WAIT_LGKM0;
    BAR;
    if (s + 4 < 20) issue(s + 4);
    const char* st = ring + (s % K1_NB) * K1_STG;
    bf16x8 xf[2];
#pragma unroll
    for (int i = 0; i < 2; ++i)
      xf[i] = *(const bf16x8*)(xs + (32 * wm + 16 * i + l15) * XS_STRIDE + (32 * s + 8 * g) * 2);
#pragma unroll
    for (int u = 0; u < 4; ++u) {
      {
        const int uc = u0 + u < 15 ? u0 + u : 14;
        const char* b = st + (8 * g + q4) * 480 + uc * 32 + p4 * 8;
        bf16x8 wf = cat8(tr_read(b), tr_read(b + 4 * 480));
#pragma unroll
        for (int i = 0; i < 2; ++i)
          acc[i][u] = __builtin_amdgcn_mfma_f32_16x16x32_bf16(wf, xf[i], acc[i][u], 0, 0, 0);
      }
    }
  }
  WAIT_LGKM0;
  BAR;
  STAMP(0, 2);

  char* pkimg = ring;
  char* vimg = ring + 64 * 192;
#pragma unroll
  for (int u = 0; u < 4; ++u) {
    const int uu = u0 + u;
    if (uu < 15) {
      const int seg = uu / 5;
      const int n0 = (uu % 5) * 16 + 4 * g;
#pragma unroll
      for (int i = 0; i < 2; ++i) {
        const int m = 32 * wm + 16 * i + l15;
        float vals[4];
#pragma unroll
        for (int r = 0; r < 4; ++r) {
          float t = bfr(acc[i][u][r]);
          if (seg < 2) { t = (t > 0.f ? t + 1.f : __expf(t)) + 1e-6f; }
          vals[r] = t;
        }
        u32x2 pk = {pack2(vals[0], vals[1]), pack2(vals[2], vals[3])};
        const int hh = n0 / 20, d0 = n0 % 20;
        if (seg == 0) {
          *(u32x2*)(pqp + ((size_t)(row0 + m) * 32 + hg * 4 + hh) * 32 + d0) = pk;
        } else if (seg == 1) {
          *(u32x2*)(pkimg + m * 192 + n0 * 2) = pk;
        } else {
          *(u32x2*)(vimg + m * 272 + (hh * 32 + d0) * 2) = pk;
        }
      }
    }
  }
  if (tid < 256) {
    const int r = tid >> 2, hh = tid & 3;
    u32x2 z2 = {0u, 0u};
    u16* dst = pqp + ((size_t)(row0 + r) * 32 + hg * 4 + hh) * 32 + 20;
    *(u32x2*)(dst) = z2; *(u32x2*)(dst + 4) = z2; *(u32x2*)(dst + 8) = z2;
    u32x2 one = {0x3F80u, 0u};
    *(u32x2*)(vimg + r * 272 + (hh * 32 + 20) * 2) = one;
  }
  __syncthreads();
  if (wave < 4) {
    const int hh = wave;
    f32x4 kv[2][2];
#pragma unroll
    for (int a = 0; a < 2; ++a)
#pragma unroll
      for (int b = 0; b < 2; ++b) kv[a][b] = (f32x4){0.f, 0.f, 0.f, 0.f};
#pragma unroll
    for (int ks = 0; ks < 2; ++ks) {
      bf16x8 af[2], bfv[2];
#pragma unroll
      for (int dt = 0; dt < 2; ++dt) {
        const char* a = pkimg + (32 * ks + 8 * g + q4) * 192 + (hh * 20 + 16 * dt + 4 * p4) * 2;
        af[dt] = cat8(tr_read(a), tr_read(a + 4 * 192));
      }
#pragma unroll
      for (int et = 0; et < 2; ++et) {
        const char* b = vimg + (32 * ks + 8 * g + q4) * 272 + (hh * 32 + 16 * et + 4 * p4) * 2;
        bfv[et] = cat8(tr_read(b), tr_read(b + 4 * 272));
      }
#pragma unroll
      for (int dt = 0; dt < 2; ++dt)
#pragma unroll
        for (int et = 0; et < 2; ++et)
          kv[dt][et] = __builtin_amdgcn_mfma_f32_16x16x32_bf16(af[dt], bfv[et], kv[dt][et], 0, 0, 0);
    }
    float* pp = part + ((size_t)(hg * 32 + rt) * 4 + hh) * 420;
#pragma unroll
    for (int dt = 0; dt < 2; ++dt)
#pragma unroll
      for (int et = 0; et < 2; ++et) {
        const int e = 16 * et + l15, d0 = 16 * dt + 4 * g;
        if (e < 21 && d0 < 20) {
#pragma unroll
          for (int r = 0; r < 4; ++r)
            __hip_atomic_store(pp + e * 20 + d0 + r, kv[dt][et][r], __ATOMIC_RELAXED, __HIP_MEMORY_SCOPE_AGENT);
        }
      }
  }
  STAMP(0, 3);
  asm volatile("s_waitcnt vmcnt(0)" ::: "memory");
  __syncthreads();
  unsigned* flag = (unsigned*)xs;
  if (tid == 0) {
    unsigned old = __hip_atomic_fetch_add(cnt + hg, 1u, __ATOMIC_RELAXED, __HIP_MEMORY_SCOPE_AGENT);
    unsigned last = (old == 31u) ? 1u : 0u;
    if (last) {
      __builtin_amdgcn_fence(__ATOMIC_ACQUIRE, "agent");
      asm volatile("s_waitcnt vmcnt(0)" ::: "memory");
    }
    *flag = last;
  }
  __syncthreads();
  if (*flag) {
    const f32x4* pb = (const f32x4*)(part + (size_t)(hg * 32) * 4 * 420);
    f32x4* red = (f32x4*)(smem + 1024);
#pragma unroll
    for (int it = 0; it < 7; it += 7) {
      f32x4 v[7][4];
#pragma unroll
      for (int k = 0; k < 7; ++k) {
        int task = (it + k) * 512 + tid;
        task = task < 3360 ? task : 3359;
        const int rg = task / 420, f = task % 420;
#pragma unroll
        for (int r = 0; r < 4; ++r) v[k][r] = pb[(size_t)(rg * 4 + r) * 420 + f];
      }
#pragma unroll
      for (int k = 0; k < 7; ++k) {
        const int task = (it + k) * 512 + tid;
        if (task < 3360) red[task] = (v[k][0] + v[k][1]) + (v[k][2] + v[k][3]);
      }
    }
    __syncthreads();
    const float* redf = (const float*)red;
    for (int o = tid; o < 4 * 1536; o += 512) {
      const int hh = o / 1536, er = (o % 1536) >> 5, d = o & 31;
      const int e = er >= 32 ? 20 : er;
      float sum = 0.f;
      if (e < 21 && d < 20) {
        const int idx = hh * 420 + e * 20 + d;
#pragma unroll
        for (int rg = 0; rg < 8; ++rg) sum += redf[rg * 1680 + idx];
      }
      kvf[(size_t)(hg * 4 + hh) * 1536 + er * 32 + d] = f2bf(sum);
    }
  }
  STAMP(0, 4);
}

constexpr int K2_STG = 32 * 160;
constexpr int K2_NB = 15;
__global__ __launch_bounds__(512, 1) void k2_attn_out(const u16* __restrict__ x, const u16* __restrict__ wo,
                                                      const u16* __restrict__ pqp, const u16* __restrict__ kvf,
                                                      u16* __restrict__ x1) {
  __shared__ __attribute__((aligned(16))) char smem[XS_BYTES + K2_NB * K2_STG];
  char* xs = smem;
  char* ring = smem + XS_BYTES;
  const int tid = threadIdx.x, lane = tid & 63, wave = __builtin_amdgcn_readfirstlane(tid >> 6);
  const int ct = (blockIdx.x >> 3) & 7, rt = (blockIdx.x & 7) * 4 + (blockIdx.x >> 6);
  const int row0 = rt * 64;
  const int g = lane >> 4, q4 = (lane & 15) >> 2, p4 = lane & 3, l15 = lane & 15;

  auto issue = [&](int s) {
    char* dst = ring + (s % K2_NB) * K2_STG;
    {
      const int j = wave;
      if (j < 5) {
        const int ci = j * 64 + lane;
        const int r = ci / 10, c = ci % 10;
        glds16(wo + (size_t)(s * 32 + r) * 640 + ct * 80 + c * 8, dst + j * 1024);
      }
    }
  };
  STAMP(1, 0);
#pragma unroll
  for (int s = 0; s < K2_NB - 1; ++s) issue(s);

  {
    bf16x8 kf[4][3], pf[4][4];
#pragma unroll
    for (int hi = 0; hi < 4; ++hi) {
      const int h = wave * 4 + hi;
#pragma unroll
      for (int et = 0; et < 3; ++et) kf[hi][et] = *(const bf16x8*)(kvf + (size_t)h * 1536 + (16 * et + l15) * 32 + 8 * g);
#pragma unroll
      for (int i = 0; i < 4; ++i) pf[hi][i] = *(const bf16x8*)(pqp + ((size_t)(row0 + 16 * i + l15) * 32 + h) * 32 + 8 * g);
    }
#pragma unroll
    for (int hi = 0; hi < 4; ++hi) {
      const int h = wave * 4 + hi;
#pragma unroll
      for (int i = 0; i < 4; ++i) {
        const int m = 16 * i + l15;
        const f32x4 zz = {0.f, 0.f, 0.f, 0.f};
        f32x4 a0 = __builtin_amdgcn_mfma_f32_16x16x32_bf16(kf[hi][0], pf[hi][i], zz, 0, 0, 0);
        f32x4 a1 = __builtin_amdgcn_mfma_f32_16x16x32_bf16(kf[hi][1], pf[hi][i], zz, 0, 0, 0);
        f32x4 ad = __builtin_amdgcn_mfma_f32_16x16x32_bf16(kf[hi][2], pf[hi][i], zz, 0, 0, 0);
        const float inv = __frcp_rn(ad[0] + 1e-6f);
        u32x2 o0 = {pack2(a0[0] * inv, a0[1] * inv), pack2(a0[2] * inv, a0[3] * inv)};
        *(u32x2*)(xs + m * XS_STRIDE + (h * 20 + 4 * g) * 2) = o0;
        if (g == 0) {
          u32x2 o1 = {pack2(a1[0] * inv, a1[1] * inv), pack2(a1[2] * inv, a1[3] * inv)};
          *(u32x2*)(xs + m * XS_STRIDE + (h * 20 + 16) * 2) = o1;
        }
      }
    }
  }

  STAMP(1, 1);
  const int wm = wave & 3, wn = wave >> 2;
  const int u0 = wn * 3;
  const int m = row0 + 16 * wm + l15;
  u32x2 xr[3];
#pragma unroll
  for (int u = 0; u < 3; ++u) {
    const int uu = u0 + u < 5 ? u0 + u : 4;
    xr[u] = *(const u32x2*)(x + (size_t)m * 640 + ct * 80 + uu * 16 + 4 * g);
  }
  f32x4 acc[3];
#pragma unroll
  for (int u = 0; u < 3; ++u) acc[u] = (f32x4){0.f, 0.f, 0.f, 0.f};
  for (int s = 0; s < 20; ++s) {
    if (s + K2_NB - 2 < 20) { if (wave < 5) wait_vm<13>(); } else wait_vm<0>();
    WAIT_LGKM0;
    BAR;
    if (s + K2_NB - 1 < 20) issue(s + K2_NB - 1);
    const char* st = ring + (s % K2_NB) * K2_STG;
    bf16x8 xf = *(const bf16x8*)(xs + (16 * wm + l15) * XS_STRIDE + (32 * s + 8 * g) * 2);
#pragma unroll
    for (int u = 0; u < 3; ++u) {
      {
        const int uc = u0 + u < 5 ? u0 + u : 4;
        const char* b = st + (8 * g + q4) * 160 + uc * 32 + p4 * 8;
        bf16x8 wf = cat8(tr_read(b), tr_read(b + 4 * 160));
        acc[u] = __builtin_amdgcn_mfma_f32_16x16x32_bf16(wf, xf, acc[u], 0, 0, 0);
      }
    }
  }
  STAMP(1, 2);
#pragma unroll
  for (int u = 0; u < 3; ++u) {
    if (u0 + u < 5) {
      const int n0 = ct * 80 + (u0 + u) * 16 + 4 * g;
      float r0 = __uint_as_float(xr[u][0] << 16) + bfr(acc[u][0]);
      float r1 = __uint_as_float(xr[u][0] & 0xFFFF0000u) + bfr(acc[u][1]);
      float r2 = __uint_as_float(xr[u][1] << 16) + bfr(acc[u][2]);
      float r3 = __uint_as_float(xr[u][1] & 0xFFFF0000u) + bfr(acc[u][3]);
      u32x2 o = {pack2(r0, r1), pack2(r2, r3)};
      *(u32x2*)(x1 + (size_t)m * 640 + n0) = o;
    }
  }
  STAMP(1, 3);
}

constexpr int K3_STG = 32 * 448;
constexpr int K3_NB = 5;
__global__ __launch_bounds__(512, 1) void k3_mlp_up(const u16* __restrict__ x1, const u16* __restrict__ w1,
                                                    const u16* __restrict__ b1, u16* __restrict__ hbuf) {
  __shared__ __attribute__((aligned(16))) char smem[XS_BYTES + K3_NB * K3_STG];
  char* xs = smem;
  char* ring = smem + XS_BYTES;
  const int tid = threadIdx.x, lane = tid & 63, wave = __builtin_amdgcn_readfirstlane(tid >> 6);
  const int ct = (blockIdx.x >> 3) & 7, rt = (blockIdx.x & 7) * 4 + (blockIdx.x >> 6);
  const int row0 = rt * 64;
  const int g = lane >> 4, q4 = (lane & 15) >> 2, p4 = lane & 3, l15 = lane & 15;

  auto issue = [&](int s) {
    char* dst = ring + (s % K3_NB) * K3_STG;
#pragma unroll
    for (int i = 0; i < 2; ++i) {
      const int j = i * 8 + wave;
      if (j < 14) {
        const int ci = j * 64 + lane;
        const int r = ci / 28, c = ci % 28;
        int col = ct * 224 + c * 8;
        col = col < F_ - 8 ? col : F_ - 8;
        glds16(w1 + (size_t)(s * 32 + r) * F_ + col, dst + j * 1024);
      }
    }
  };
  STAMP(2, 0);
  issue(0); issue(1); issue(2); issue(3);
  ln_tile(x1, row0, xs, wave, lane);
  STAMP(2, 1);

  const int wm = wave & 1, wn = wave >> 1;
  const int u0 = wn * 4;
  f32x4 acc[2][4];
#pragma unroll
  for (int i = 0; i < 2; ++i)
#pragma unroll
    for (int u = 0; u < 4; ++u) acc[i][u] = (f32x4){0.f, 0.f, 0.f, 0.f};
  u32x2 bb[4];
#pragma unroll
  for (int u = 0; u < 4; ++u) {
    int n0 = ct * 224 + (u0 + u) * 16 + 4 * g;
    n0 = (u0 + u < 14 && n0 < F_) ? n0 : 0;
    bb[u] = *(const u32x2*)(b1 + n0);
  }

  for (int s = 0; s < 20; ++s) {
    if (s + 3 < 20) { if (wave < 6) wait_vm<6>(); else wait_vm<3>(); } else wait_vm<0>();
    WAIT_LGKM0;
    BAR;
    if (s + 4 < 20) issue(s + 4);
    const char* st = ring + (s % K3_NB) * K3_STG;
    bf16x8 xf[2];
#pragma unroll
    for (int i = 0; i < 2; ++i)
      xf[i] = *(const bf16x8*)(xs + (32 * wm + 16 * i + l15) * XS_STRIDE + (32 * s + 8 * g) * 2);
#pragma unroll
    for (int u = 0; u < 4; ++u) {
      {
        const int uc = u0 + u < 14 ? u0 + u : 13;
        const char* b = st + (8 * g + q4) * 448 + uc * 32 + p4 * 8;
        bf16x8 wf = cat8(tr_read(b), tr_read(b + 4 * 448));
#pragma unroll
        for (int i = 0; i < 2; ++i)
          acc[i][u] = __builtin_amdgcn_mfma_f32_16x16x32_bf16(wf, xf[i], acc[i][u], 0, 0, 0);
      }
    }
  }
  STAMP(2, 2);
#pragma unroll
  for (int u = 0; u < 4; ++u) {
    const int n0 = ct * 224 + (u0 + u) * 16 + 4 * g;
    if (u0 + u < 14 && n0 < F_) {
      float bv[4] = {__uint_as_float(bb[u][0] << 16), __uint_as_float(bb[u][0] & 0xFFFF0000u),
                     __uint_as_float(bb[u][1] << 16), __uint_as_float(bb[u][1] & 0xFFFF0000u)};
#pragma unroll
      for (int i = 0; i < 2; ++i) {
        const int m = row0 + 32 * wm + 16 * i + l15;
        float o[4];
#pragma unroll
        for (int r = 0; r < 4; ++r) {
          float t = bfr(acc[i][u][r] + bv[r]);
          float inner = 0.7978845608028654f * (t + 0.044715f * t * t * t);
          inner = fminf(fmaxf(inner, -15.f), 15.f);
          float e2 = __expf(2.f * inner);
          float th = 1.f - 2.f * __frcp_rn(e2 + 1.f);
          o[r] = 0.5f * t * (1.0f + th);
        }
        u32x2 ov = {pack2(o[0], o[1]), pack2(o[2], o[3])};
        *(u32x2*)(hbuf + (size_t)m * F_ + n0) = ov;
      }
    }
  }
  STAMP(2, 3);
}

constexpr int K4_A = 64 * 128;
constexpr int K4_B = 64 * 160;
constexpr int K4_STG = K4_A + K4_B;
constexpr int K4_NB = 6;
constexpr int K4_NS = 27;
__global__ __launch_bounds__(256, 1) void k4_mlp_down(const u16* __restrict__ hbuf, const u16* __restrict__ w2,
                                                      const u16* __restrict__ b2, const u16* __restrict__ x1,
                                                      u16* __restrict__ xo) {
  __shared__ __attribute__((aligned(16))) char smem[K4_NB * K4_STG];
  const int tid = threadIdx.x, lane = tid & 63, wave = __builtin_amdgcn_readfirstlane(tid >> 6);
  const int ct = (blockIdx.x >> 3) & 7, rt = (blockIdx.x & 7) * 4 + (blockIdx.x >> 6);
  const int row0 = rt * 64;
  const int g = lane >> 4, q4 = (lane & 15) >> 2, p4 = lane & 3, l15 = lane & 15;

  auto issue = [&](int s) {
    char* base = smem + (s % K4_NB) * K4_STG;
#pragma unroll
    for (int i = 0; i < 2; ++i) {
      const int r = 16 * wave + 8 * i + (lane >> 3), c = (lane & 7) ^ (r & 7);
      glds16(hbuf + (size_t)(row0 + r) * F_ + s * 64 + c * 8, base + (16 * wave + 8 * i) * 128);
    }
    char* dst = base + K4_A;
#pragma unroll
    for (int i = 0; i < 3; ++i) {
      const int j = i * 4 + wave;
      if (j < 10) {
        const int ci = j * 64 + lane;
        const int r = ci / 10, c = ci % 10;
        glds16(w2 + (size_t)(s * 64 + r) * 640 + ct * 80 + c * 8, dst + j * 1024);
      }
    }
  };
  STAMP(3, 0);
#pragma unroll
  for (int s = 0; s < K4_NB - 1; ++s) issue(s);
  STAMP(3, 1);

  f32x4 acc[5];
#pragma unroll
  for (int u = 0; u < 5; ++u) acc[u] = (f32x4){0.f, 0.f, 0.f, 0.f};
  for (int s = 0; s < K4_NS; ++s) {
    if (s + K4_NB - 2 < K4_NS) { if (wave < 2) wait_vm<20>(); else wait_vm<16>(); } else wait_vm<0>();
    WAIT_LGKM0;
    BAR;
    if (s + K4_NB - 1 < K4_NS) issue(s + K4_NB - 1);
    const char* st = smem + (s % K4_NB) * K4_STG;
#pragma unroll
    for (int kk = 0; kk < 2; ++kk) {
      bf16x8 xf = *(const bf16x8*)(st + (16 * wave + l15) * 128 + (((4 * kk + g) ^ (l15 & 7)) * 16));
#pragma unroll
      for (int u = 0; u < 5; ++u) {
        const char* b = st + K4_A + (32 * kk + 8 * g + q4) * 160 + u * 32 + p4 * 8;
        bf16x8 wf = cat8(tr_read(b), tr_read(b + 4 * 160));
        acc[u] = __builtin_amdgcn_mfma_f32_16x16x32_bf16(wf, xf, acc[u], 0, 0, 0);
      }
    }
  }
  STAMP(3, 2);
  const int m = row0 + 16 * wave + l15;
#pragma unroll
  for (int u = 0; u < 5; ++u) {
    const int n0 = ct * 80 + u * 16 + 4 * g;
    u32x2 xr = *(const u32x2*)(x1 + (size_t)m * 640 + n0);
    u32x2 bb = *(const u32x2*)(b2 + n0);
    float r0 = __uint_as_float(xr[0] << 16) + bfr(bfr(acc[u][0]) + __uint_as_float(bb[0] << 16));
    float r1 = __uint_as_float(xr[0] & 0xFFFF0000u) + bfr(bfr(acc[u][1]) + __uint_as_float(bb[0] & 0xFFFF0000u));
    float r2 = __uint_as_float(xr[1] << 16) + bfr(bfr(acc[u][2]) + __uint_as_float(bb[1] << 16));
    float r3 = __uint_as_float(xr[1] & 0xFFFF0000u) + bfr(bfr(acc[u][3]) + __uint_as_float(bb[1] & 0xFFFF0000u));
    u32x2 o = {pack2(r0, r1), pack2(r2, r3)};
    *(u32x2*)(xo + (size_t)m * 640 + n0) = o;
  }
  STAMP(3, 3);
}

extern "C" void kernel_launch(void* const* d_in, const int* in_sizes, int n_in,
                              void* d_out, int out_size, void* d_ws, size_t ws_size,
                              hipStream_t stream) {
  const u16* A_x = (const u16*)d_in[0];
  const u16* wqkv = (const u16*)d_in[1];
  const u16* wo = (const u16*)d_in[2];
  const u16* w1 = (const u16*)d_in[4];
  const u16* b1 = (const u16*)d_in[5];
  const u16* w2 = (const u16*)d_in[6];
  const u16* b2 = (const u16*)d_in[7];
  char* ws = (char*)d_ws;
  unsigned* cnt = (unsigned*)ws;
  u16* pqp = (u16*)(ws + 4096);
  float* part = (float*)(ws + 4096 + 4194304);
  u16* kvf = (u16*)(ws + 4096 + 4194304 + 1720320);
  u16* x1 = (u16*)(ws + 4096 + 4194304 + 1720320 + 131072);
  u16* x2 = x1 + (size_t)S_ * D_;
  u16* hbuf = x2 + (size_t)S_ * D_;
  hipMemsetAsync(cnt, 0, 64, stream);
  const u16* xin = A_x;
  for (int layer = 0; layer < NLAYERS; ++layer) {
    u16* xout = layer == 0 ? x2 : (u16*)d_out;
    k1_qkv<<<256, 512, 0, stream>>>(xin, wqkv, pqp, part, kvf, cnt + 8 * layer);
    k2_attn_out<<<256, 512, 0, stream>>>(xin, wo, pqp, kvf, x1);
    k3_mlp_up<<<256, 512, 0, stream>>>(x1, w1, b1, hbuf);
    k4_mlp_down<<<256, 256, 0, stream>>>(hbuf, w2, b2, x1, xout);
    xin = x2;
  }
}
```

```cpp
#include <hip/hip_runtime.h>
#include <hip/hip_bf16.h>
#include <stdint.h>
#define NLAYERS 2

typedef __attribute__((ext_vector_type(8))) short bf16x8;
typedef __attribute__((ext_vector_type(4))) short s16x4;
typedef __attribute__((ext_vector_type(4))) float f32x4;
typedef __attribute__((ext_vector_type(2))) unsigned u32x2;
typedef __attribute__((ext_vector_type(4))) unsigned u32x4;
typedef unsigned short u16;
#define LDS_AS __attribute__((address_space(3)))

constexpr int S_ = 2048, D_ = 640, F_ = 1728;
constexpr int XS_STRIDE = 1296;
constexpr int XS_BYTES = 64 * XS_STRIDE;

__device__ __forceinline__ float bf2f(u16 v) { return __uint_as_float(((unsigned)v) << 16); }
__device__ __forceinline__ u16 f2bf(float f) {
  unsigned u = __float_as_uint(f);
  u += 0x7FFFu + ((u >> 16) & 1u);
  return (u16)(u >> 16);
}
__device__ __forceinline__ unsigned pack2(float a, float b) {
  unsigned r;
  asm("v_cvt_pk_bf16_f32 %0, %1, %2" : "=v"(r) : "v"(a), "v"(b));
  return r;
}
__device__ __forceinline__ float bfr(float f) { return __uint_as_float(pack2(f, f) & 0xFFFF0000u); }

#define WAIT_VM(n) asm volatile("s_waitcnt vmcnt(" #n ")" ::: "memory")
#define WAIT_LGKM0 asm volatile("s_waitcnt lgkmcnt(0)" ::: "memory")
#define BAR __builtin_amdgcn_s_barrier()
#define STAMP(k, i)

template <int N> __device__ __forceinline__ void wait_vm() {
  if constexpr (N == 0) WAIT_VM(0);
  else if constexpr (N == 2) WAIT_VM(2);
  else if constexpr (N == 3) WAIT_VM(3);
  else if constexpr (N == 4) WAIT_VM(4);
  else if constexpr (N == 6) WAIT_VM(6);
  else if constexpr (N == 8) WAIT_VM(8);
  else if constexpr (N == 9) WAIT_VM(9);
  else if constexpr (N == 12) WAIT_VM(12);
  else if constexpr (N == 14) WAIT_VM(14);
  else if constexpr (N == 15) WAIT_VM(15);
  else if constexpr (N == 18) WAIT_VM(18);
  else if constexpr (N == 20) WAIT_VM(20);
  else if constexpr (N == 24) WAIT_VM(24);
  else if constexpr (N == 1) WAIT_VM(1);
  else if constexpr (N == 13) WAIT_VM(13);
  else if constexpr (N == 16) WAIT_VM(16);
  else if constexpr (N == 10) WAIT_VM(10);
  else WAIT_VM(0);
}

__device__ __forceinline__ void glds16(const void* g, char* lds_base) {
  unsigned keep;
  const unsigned base = __builtin_amdgcn_readfirstlane((unsigned)(size_t)lds_base);
  asm volatile("s_mov_b32 %0, m0\n\ts_mov_b32 m0, %2\n\ts_nop 0\n\tglobal_load_lds_dwordx4 %1, off\n\ts_mov_b32 m0, %0"
               : "=&s"(keep) : "v"(g), "s"(base) : "memory");
}
__device__ __forceinline__ s16x4 tr_read(const char* p) {
  return __builtin_amdgcn_ds_read_tr16_b64_v4i16((LDS_AS s16x4*)p);
}
__device__ __forceinline__ bf16x8 cat8(s16x4 a, s16x4 b) {
  bf16x8 r;
  r[0] = a[0]; r[1] = a[1]; r[2] = a[2]; r[3] = a[3];
  r[4] = b[0]; r[5] = b[1]; r[6] = b[2]; r[7] = b[3];
  return r;
}

__device__ __forceinline__ void ln_tile(const u16* __restrict__ x, int row0, char* xs, int wave, int lane) {
  const int j = lane & 15, rsub = lane >> 4;
  u32x4 q[2][5];
#pragma unroll
  for (int pass = 0; pass < 2; ++pass) {
    const u16* src = x + (size_t)(row0 + wave * 8 + pass * 4 + rsub) * D_;
#pragma unroll
    for (int c = 0; c < 5; ++c) q[pass][c] = *(const u32x4*)(src + (j + 16 * c) * 8);
  }
#pragma unroll
  for (int pass = 0; pass < 2; ++pass) {
    const int r = wave * 8 + pass * 4 + rsub;
    float v[40];
#pragma unroll
    for (int c = 0; c < 5; ++c) {
#pragma unroll
      for (int e = 0; e < 4; ++e) {
        v[c * 8 + 2 * e] = __uint_as_float(q[pass][c][e] << 16);
        v[c * 8 + 2 * e + 1] = __uint_as_float(q[pass][c][e] & 0xFFFF0000u);
      }
    }
    float s = 0.f;
#pragma unroll
    for (int e = 0; e < 40; ++e) s += v[e];
    s += __shfl_xor(s, 1); s += __shfl_xor(s, 2); s += __shfl_xor(s, 4); s += __shfl_xor(s, 8);
    const float mean = s * (1.f / 640.f);
    float q2 = 0.f;
#pragma unroll
    for (int e = 0; e < 40; ++e) { float d = v[e] - mean; q2 += d * d; }
    q2 += __shfl_xor(q2, 1); q2 += __shfl_xor(q2, 2); q2 += __shfl_xor(q2, 4); q2 += __shfl_xor(q2, 8);
    const float rstd = __frsqrt_rn(q2 * (1.f / 640.f) + 1e-5f);
    const float nmr = -mean * rstd;
#pragma unroll
    for (int c = 0; c < 5; ++c) {
      u32x4 o;
#pragma unroll
      for (int e = 0; e < 4; ++e)
        o[e] = pack2(fmaf(v[c * 8 + 2 * e], rstd, nmr), fmaf(v[c * 8 + 2 * e + 1], rstd, nmr));
      *(u32x4*)(xs + r * XS_STRIDE + (j + 16 * c) * 16) = o;
    }
  }
}

constexpr int K1_STG = 32 * 480;
constexpr int K1_NB = 5;
__global__ __launch_bounds__(512, 1) void k1_qkv(const u16* __restrict__ x, const u16* __restrict__ wqkv,
                                                 u16* __restrict__ pqp, float* __restrict__ part,
                                                 u16* __restrict__ kvf, unsigned* __restrict__ cnt) {
  __shared__ __attribute__((aligned(16))) char smem[XS_BYTES + K1_NB * K1_STG];
  char* xs = smem;
  char* ring = smem + XS_BYTES;
  const int tid = threadIdx.x, lane = tid & 63, wave = __builtin_amdgcn_readfirstlane(tid >> 6);
  const int hg = (blockIdx.x >> 3) & 7, rt = (blockIdx.x & 7) * 4 + (blockIdx.x >> 6);
  const int row0 = rt * 64;
  const int g = lane >> 4, q4 = (lane & 15) >> 2, p4 = lane & 3, l15 = lane & 15;

  auto issue = [&](int s) {
    char* dst = ring + (s % K1_NB) * K1_STG;
#pragma unroll
    for (int i = 0; i < 2; ++i) {
      const int j = i * 8 + wave;
      if (j < 15) {
        const int ci = j * 64 + lane;
        const int r = ci / 30, c = ci % 30;
        const int col = (c / 10) * 640 + hg * 80 + (c % 10) * 8;
        glds16(wqkv + (size_t)(s * 32 + r) * 1920 + col, dst + j * 1024);
      }
    }
  };
  STAMP(0, 0);
  issue(0); issue(1); issue(2); issue(3);
  ln_tile(x, row0, xs, wave, lane);
  STAMP(0, 1);

  const int wm = wave & 1, wn = wave >> 1;
  const int u0 = wn * 4;
  f32x4 acc[2][4];
#pragma unroll
  for (int i = 0; i < 2; ++i)
#pragma unroll
    for (int u = 0; u < 4; ++u) acc[i][u] = (f32x4){0.f, 0.f, 0.f, 0.f};

  auto rd = [&](int s, bf16x8 (&xf)[2], bf16x8 (&wf)[4]) {
    const char* st = ring + (s % K1_NB) * K1_STG;
#pragma unroll
    for (int i = 0; i < 2; ++i)
      xf[i] = *(const bf16x8*)(xs + (32 * wm + 16 * i + l15) * XS_STRIDE + (32 * s + 8 * g) * 2);
#pragma unroll
    for (int u = 0; u < 4; ++u) {
      const int uc = u0 + u < 15 ? u0 + u : 14;
      const char* b = st + (8 * g + q4) * 480 + uc * 32 + p4 * 8;
      wf[u] = cat8(tr_read(b), tr_read(b + 4 * 480));
    }
  };
  auto mm = [&](bf16x8 (&xf)[2], bf16x8 (&wf)[4]) {
#pragma unroll
    for (int u = 0; u < 4; ++u)
#pragma unroll
      for (int i = 0; i < 2; ++i)
        acc[i][u] = __builtin_amdgcn_mfma_f32_16x16x32_bf16(wf[u], xf[i], acc[i][u], 0, 0, 0);
  };
  auto sync = [&](int s) {
    if (s + K1_NB - 1 < 20) { if (wave < 7) wait_vm<6>(); else wait_vm<3>(); } else wait_vm<0>();
    WAIT_LGKM0;
    BAR;
    if (s >= 0 && s + K1_NB < 20) issue(s + K1_NB);
  };
  issue(4);
  bf16x8 xa[2], wa[4], xb[2], wb[4];
  sync(-1);
  rd(0, xa, wa);
  for (int s = 0; s < 20; s += 2) {
    sync(s);
    rd(s + 1, xb, wb);
    mm(xa, wa);
    sync(s + 1);
    if (s + 2 < 20) rd(s + 2, xa, wa);
    mm(xb, wb);
  }
  WAIT_LGKM0;
  BAR;
  STAMP(0, 2);

  char* pkimg = ring;
  char* vimg = ring + 64 * 192;
#pragma unroll
  for (int u = 0; u < 4; ++u) {
    const int uu = u0 + u;
    if (uu < 15) {
      const int seg = uu / 5;
      const int n0 = (uu % 5) * 16 + 4 * g;
#pragma unroll
      for (int i = 0; i < 2; ++i) {
        const int m = 32 * wm + 16 * i + l15;
        float vals[4];
#pragma unroll
        for (int r = 0; r < 4; ++r) {
          float t = bfr(acc[i][u][r]);
          if (seg < 2) { t = (t > 0.f ? t + 1.f : __expf(t)) + 1e-6f; }
          vals[r] = t;
        }
        u32x2 pk = {pack2(vals[0], vals[1]), pack2(vals[2], vals[3])};
        const int hh = n0 / 20, d0 = n0 % 20;
        if (seg == 0) {
          *(u32x2*)(pqp + ((size_t)(row0 + m) * 32 + hg * 4 + hh) * 32 + d0) = pk;
        } else if (seg == 1) {
          *(u32x2*)(pkimg + m * 192 + n0 * 2) = pk;
        } else {
          *(u32x2*)(vimg + m * 272 + (hh * 32 + d0) * 2) = pk;
        }
      }
    }
  }
  if (tid < 256) {
    const int r = tid >> 2, hh = tid & 3;
    u32x2 z2 = {0u, 0u};
    u16* dst = pqp + ((size_t)(row0 + r) * 32 + hg * 4 + hh) * 32 + 20;
    *(u32x2*)(dst) = z2; *(u32x2*)(dst + 4) = z2; *(u32x2*)(dst + 8) = z2;
    u32x2 one = {0x3F80u, 0u};
    *(u32x2*)(vimg + r * 272 + (hh * 32 + 20) * 2) = one;
  }
  __syncthreads();
  if (wave < 4) {
    const int hh = wave;
    f32x4 kv[2][2];
#pragma unroll
    for (int a = 0; a < 2; ++a)
#pragma unroll
      for (int b = 0; b < 2; ++b) kv[a][b] = (f32x4){0.f, 0.f, 0.f, 0.f};
#pragma unroll
    for (int ks = 0; ks < 2; ++ks) {
      bf16x8 af[2], bfv[2];
#pragma unroll
      for (int dt = 0; dt < 2; ++dt) {
        const char* a = pkimg + (32 * ks + 8 * g + q4) * 192 + (hh * 20 + 16 * dt + 4 * p4) * 2;
        af[dt] = cat8(tr_read(a), tr_read(a + 4 * 192));
      }
#pragma unroll
      for (int et = 0; et < 2; ++et) {
        const char* b = vimg + (32 * ks + 8 * g + q4) * 272 + (hh * 32 + 16 * et + 4 * p4) * 2;
        bfv[et] = cat8(tr_read(b), tr_read(b + 4 * 272));
      }
#pragma unroll
      for (int dt = 0; dt < 2; ++dt)
#pragma unroll
        for (int et = 0; et < 2; ++et)
          kv[dt][et] = __builtin_amdgcn_mfma_f32_16x16x32_bf16(af[dt], bfv[et], kv[dt][et], 0, 0, 0);
    }
    float* pp = part + ((size_t)(hg * 32 + rt) * 4 + hh) * 420;
#pragma unroll
    for (int dt = 0; dt < 2; ++dt)
#pragma unroll
      for (int et = 0; et < 2; ++et) {
        const int e = 16 * et + l15, d0 = 16 * dt + 4 * g;
        if (e < 21 && d0 < 20) {
#pragma unroll
          for (int r = 0; r < 4; ++r)
            __hip_atomic_store(pp + e * 20 + d0 + r, kv[dt][et][r], __ATOMIC_RELAXED, __HIP_MEMORY_SCOPE_AGENT);
        }
      }
  }
  STAMP(0, 3);
  asm volatile("s_waitcnt vmcnt(0)" ::: "memory");
  __syncthreads();
  unsigned* flag = (unsigned*)xs;
  if (tid == 0) {
    unsigned old = __hip_atomic_fetch_add(cnt + hg, 1u, __ATOMIC_RELAXED, __HIP_MEMORY_SCOPE_AGENT);
    unsigned last = (old == 31u) ? 1u : 0u;
    if (last) {
      __builtin_amdgcn_fence(__ATOMIC_ACQUIRE, "agent");
      asm volatile("s_waitcnt vmcnt(0)" ::: "memory");
    }
    *flag = last;
  }
  __syncthreads();
  if (*flag) {
    const f32x4* pb = (const f32x4*)(part + (size_t)(hg * 32) * 4 * 420);
    f32x4* red = (f32x4*)(smem + 1024);
    {
      f32x4 v[7][4];
#pragma unroll
      for (int k = 0; k < 7; ++k) {
        int task = k * 512 + tid;
        task = task < 3360 ? task : 3359;
        const int rg = task / 420, f = task % 420;
#pragma unroll
        for (int r = 0; r < 4; ++r) v[k][r] = pb[(size_t)(rg * 4 + r) * 420 + f];
      }
#pragma unroll
      for (int k = 0; k < 7; ++k) {
        const int task = k * 512 + tid;
        if (task < 3360) red[task] = (v[k][0] + v[k][1]) + (v[k][2] + v[k][3]);
      }
    }
    __syncthreads();
    const float* redf = (const float*)red;
    for (int o = tid; o < 4 * 1536; o += 512) {
      const int hh = o / 1536, er = (o % 1536) >> 5, d = o & 31;
      const int e = er >= 32 ? 20 : er;
      float sum = 0.f;
      if (e < 21 && d < 20) {
        const int idx = hh * 420 + e * 20 + d;
#pragma unroll
        for (int rg = 0; rg < 8; ++rg) sum += redf[rg * 1680 + idx];
      }
      kvf[(size_t)(hg * 4 + hh) * 1536 + er * 32 + d] = f2bf(sum);
    }
  }
  STAMP(0, 4);
}

constexpr int K2_STG = 32 * 160;
constexpr int K2_NB = 15;
__global__ __launch_bounds__(512, 1) void k2_attn_out(const u16* __restrict__ x, const u16* __restrict__ wo,
                                                      const u16* __restrict__ pqp, const u16* __restrict__ kvf,
                                                      u16* __restrict__ x1) {
  __shared__ __attribute__((aligned(16))) char smem[XS_BYTES + K2_NB * K2_STG];
  char* xs = smem;
  char* ring = smem + XS_BYTES;
  const int tid = threadIdx.x, lane = tid & 63, wave = __builtin_amdgcn_readfirstlane(tid >> 6);
  const int ct = (blockIdx.x >> 3) & 7, rt = (blockIdx.x & 7) * 4 + (blockIdx.x >> 6);
  const int row0 = rt * 64;
  const int g = lane >> 4, q4 = (lane & 15) >> 2, p4 = lane & 3, l15 = lane & 15;

  auto issue = [&](int s) {
    char* dst = ring + (s % K2_NB) * K2_STG;
    {
      const int j = wave;
      if (j < 5) {
        const int ci = j * 64 + lane;
        const int r = ci / 10, c = ci % 10;
        glds16(wo + (size_t)(s * 32 + r) * 640 + ct * 80 + c * 8, dst + j * 1024);
      }
    }
  };
  STAMP(1, 0);
#pragma unroll
  for (int s = 0; s < K2_NB - 1; ++s) issue(s);

  {
    bf16x8 kf[4][3], pf[4][4];
#pragma unroll
    for (int hi = 0; hi < 4; ++hi) {
      const int h = wave * 4 + hi;
#pragma unroll
      for (int et = 0; et < 3; ++et) kf[hi][et] = *(const bf16x8*)(kvf + (size_t)h * 1536 + (16 * et + l15) * 32 + 8 * g);
#pragma unroll
      for (int i = 0; i < 4; ++i) pf[hi][i] = *(const bf16x8*)(pqp + ((size_t)(row0 + 16 * i + l15) * 32 + h) * 32 + 8 * g);
    }
#pragma unroll
    for (int hi = 0; hi < 4; ++hi) {
      const int h = wave * 4 + hi;
#pragma unroll
      for (int i = 0; i < 4; ++i) {
        const int m = 16 * i + l15;
        const f32x4 zz = {0.f, 0.f, 0.f, 0.f};
        f32x4 a0 = __builtin_amdgcn_mfma_f32_16x16x32_bf16(kf[hi][0], pf[hi][i], zz, 0, 0, 0);
        f32x4 a1 = __builtin_amdgcn_mfma_f32_16x16x32_bf16(kf[hi][1], pf[hi][i], zz, 0, 0, 0);
        f32x4 ad = __builtin_amdgcn_mfma_f32_16x16x32_bf16(kf[hi][2], pf[hi][i], zz, 0, 0, 0);
        const float inv = __frcp_rn(ad[0] + 1e-6f);
        u32x2 o0 = {pack2(a0[0] * inv, a0[1] * inv), pack2(a0[2] * inv, a0[3] * inv)};
        *(u32x2*)(xs + m * XS_STRIDE + (h * 20 + 4 * g) * 2) = o0;
        if (g == 0) {
          u32x2 o1 = {pack2(a1[0] * inv, a1[1] * inv), pack2(a1[2] * inv, a1[3] * inv)};
          *(u32x2*)(xs + m * XS_STRIDE + (h * 20 + 16) * 2) = o1;
        }
      }
    }
  }

  STAMP(1, 1);
  const int wm = wave & 3, wn = wave >> 2;
  const int u0 = wn * 3;
  const int m = row0 + 16 * wm + l15;
  u32x2 xr[3];
#pragma unroll
  for (int u = 0; u < 3; ++u) {
    const int uu = u0 + u < 5 ? u0 + u : 4;
    xr[u] = *(const u32x2*)(x + (size_t)m * 640 + ct * 80 + uu * 16 + 4 * g);
  }
  f32x4 acc[3];
#pragma unroll
  for (int u = 0; u < 3; ++u) acc[u] = (f32x4){0.f, 0.f, 0.f, 0.f};
  auto rd = [&](int s, bf16x8& xf, bf16x8 (&wf)[3]) {
    const char* st = ring + (s % K2_NB) * K2_STG;
    xf = *(const bf16x8*)(xs + (16 * wm + l15) * XS_STRIDE + (32 * s + 8 * g) * 2);
#pragma unroll
    for (int u = 0; u < 3; ++u) {
      const int uc = u0 + u < 5 ? u0 + u : 4;
      const char* b = st + (8 * g + q4) * 160 + uc * 32 + p4 * 8;
      wf[u] = cat8(tr_read(b), tr_read(b + 4 * 160));
    }
  };
  auto mm = [&](bf16x8& xf, bf16x8 (&wf)[3]) {
#pragma unroll
    for (int u = 0; u < 3; ++u) acc[u] = __builtin_amdgcn_mfma_f32_16x16x32_bf16(wf[u], xf, acc[u], 0, 0, 0);
  };
  auto sync = [&](int s) {
    if (s + K2_NB - 1 < 20) { if (wave < 5) wait_vm<13>(); } else wait_vm<0>();
    WAIT_LGKM0;
    BAR;
    if (s >= 0 && s + K2_NB < 20) issue(s + K2_NB);
  };
  issue(K2_NB - 1);
  bf16x8 xa, wa[3], xb, wb[3];
  sync(-1);
  rd(0, xa, wa);
  for (int s = 0; s < 20; s += 2) {
    sync(s);
    rd(s + 1, xb, wb);
    mm(xa, wa);
    sync(s + 1);
    if (s + 2 < 20) rd(s + 2, xa, wa);
    mm(xb, wb);
  }
  STAMP(1, 2);
#pragma unroll
  for (int u = 0; u < 3; ++u) {
    if (u0 + u < 5) {
      const int n0 = ct * 80 + (u0 + u) * 16 + 4 * g;
      float r0 = __uint_as_float(xr[u][0] << 16) + bfr(acc[u][0]);
      float r1 = __uint_as_float(xr[u][0] & 0xFFFF0000u) + bfr(acc[u][1]);
      float r2 = __uint_as_float(xr[u][1] << 16) + bfr(acc[u][2]);
      float r3 = __uint_as_float(xr[u][1] & 0xFFFF0000u) + bfr(acc[u][3]);
      u32x2 o = {pack2(r0, r1), pack2(r2, r3)};
      *(u32x2*)(x1 + (size_t)m * 640 + n0) = o;
    }
  }
  STAMP(1, 3);
}

constexpr int K3_STG = 32 * 448;
constexpr int K3_NB = 5;
__global__ __launch_bounds__(512, 1) void k3_mlp_up(const u16* __restrict__ x1, const u16* __restrict__ w1,
                                                    const u16* __restrict__ b1, u16* __restrict__ hbuf) {
  __shared__ __attribute__((aligned(16))) char smem[XS_BYTES + K3_NB * K3_STG];
  char* xs = smem;
  char* ring = smem + XS_BYTES;
  const int tid = threadIdx.x, lane = tid & 63, wave = __builtin_amdgcn_readfirstlane(tid >> 6);
  const int ct = (blockIdx.x >> 3) & 7, rt = (blockIdx.x & 7) * 4 + (blockIdx.x >> 6);
  const int row0 = rt * 64;
  const int g = lane >> 4, q4 = (lane & 15) >> 2, p4 = lane & 3, l15 = lane & 15;

  auto issue = [&](int s) {
    char* dst = ring + (s % K3_NB) * K3_STG;
#pragma unroll
    for (int i = 0; i < 2; ++i) {
      const int j = i * 8 + wave;
      if (j < 14) {
        const int ci = j * 64 + lane;
        const int r = ci / 28, c = ci % 28;
        int col = ct * 224 + c * 8;
        col = col < F_ - 8 ? col : F_ - 8;
        glds16(w1 + (size_t)(s * 32 + r) * F_ + col, dst + j * 1024);
      }
    }
  };
  STAMP(2, 0);
  issue(0); issue(1); issue(2); issue(3);
  ln_tile(x1, row0, xs, wave, lane);
  STAMP(2, 1);

  const int wm = wave & 1, wn = wave >> 1;
  const int u0 = wn * 4;
  f32x4 acc[2][4];
#pragma unroll
  for (int i = 0; i < 2; ++i)
#pragma unroll
    for (int u = 0; u < 4; ++u) acc[i][u] = (f32x4){0.f, 0.f, 0.f, 0.f};
  u32x2 bb[4];
#pragma unroll
  for (int u = 0; u < 4; ++u) {
    int n0 = ct * 224 + (u0 + u) * 16 + 4 * g;
    n0 = (u0 + u < 14 && n0 < F_) ? n0 : 0;
    bb[u] = *(const u32x2*)(b1 + n0);
  }

  auto rd = [&](int s, bf16x8 (&xf)[2], bf16x8 (&wf)[4]) {
    const char* st = ring + (s % K3_NB) * K3_STG;
#pragma unroll
    for (int i = 0; i < 2; ++i)
      xf[i] = *(const bf16x8*)(xs + (32 * wm + 16 * i + l15) * XS_STRIDE + (32 * s + 8 * g) * 2);
#pragma unroll
    for (int u = 0; u < 4; ++u) {
      const int uc = u0 + u < 14 ? u0 + u : 13;
      const char* b = st + (8 * g + q4) * 448 + uc * 32 + p4 * 8;
      wf[u] = cat8(tr_read(b), tr_read(b + 4 * 448));
    }
  };
  auto mm = [&](bf16x8 (&xf)[2], bf16x8 (&wf)[4]) {
#pragma unroll
    for (int u = 0; u < 4; ++u)
#pragma unroll
      for (int i = 0; i < 2; ++i)
        acc[i][u] = __builtin_amdgcn_mfma_f32_16x16x32_bf16(wf[u], xf[i], acc[i][u], 0, 0, 0);
  };
  auto sync = [&](int s) {
    if (s + K3_NB - 1 < 20) { if (wave < 6) wait_vm<6>(); else wait_vm<3>(); } else wait_vm<0>();
    WAIT_LGKM0;
    BAR;
    if (s >= 0 && s + K3_NB < 20) issue(s + K3_NB);
  };
  issue(4);
  bf16x8 xa[2], wa[4], xb[2], wb[4];
  sync(-1);
  rd(0, xa, wa);
  for (int s = 0; s < 20; s += 2) {
    sync(s);
    rd(s + 1, xb, wb);
    mm(xa, wa);
    sync(s + 1);
    if (s + 2 < 20) rd(s + 2, xa, wa);
    mm(xb, wb);
  }
  STAMP(2, 2);
#pragma unroll
  for (int u = 0; u < 4; ++u) {
    const int n0 = ct * 224 + (u0 + u) * 16 + 4 * g;
    if (u0 + u < 14 && n0 < F_) {
      float bv[4] = {__uint_as_float(bb[u][0] << 16), __uint_as_float(bb[u][0] & 0xFFFF0000u),
                     __uint_as_float(bb[u][1] << 16), __uint_as_float(bb[u][1] & 0xFFFF0000u)};
#pragma unroll
      for (int i = 0; i < 2; ++i) {
        const int m = row0 + 32 * wm + 16 * i + l15;
        float o[4];
#pragma unroll
        for (int r = 0; r < 4; ++r) {
          float t = bfr(acc[i][u][r] + bv[r]);
          float inner = 0.7978845608028654f * (t + 0.044715f * t * t * t);
          inner = fminf(fmaxf(inner, -15.f), 15.f);
          float e2 = __expf(2.f * inner);
          float th = 1.f - 2.f * __frcp_rn(e2 + 1.f);
          o[r] = 0.5f * t * (1.0f + th);
        }
        u32x2 ov = {pack2(o[0], o[1]), pack2(o[2], o[3])};
        *(u32x2*)(hbuf + (size_t)m * F_ + n0) = ov;
      }
    }
  }
  STAMP(2, 3);
}

constexpr int K4_A = 64 * 128;
constexpr int K4_B = 64 * 160;
constexpr int K4_STG = K4_A + K4_B;
constexpr int K4_NB = 6;
constexpr int K4_NS = 27;
__global__ __launch_bounds__(256, 1) void k4_mlp_down(const u16* __restrict__ hbuf, const u16* __restrict__ w2,
                                                      const u16* __restrict__ b2, const u16* __restrict__ x1,
                                                      u16* __restrict__ xo) {
  __shared__ __attribute__((aligned(16))) char smem[K4_NB * K4_STG];
  const int tid = threadIdx.x, lane = tid & 63, wave = __builtin_amdgcn_readfirstlane(tid >> 6);
  const int ct = (blockIdx.x >> 3) & 7, rt = (blockIdx.x & 7) * 4 + (blockIdx.x >> 6);
  const int row0 = rt * 64;
  const int g = lane >> 4, q4 = (lane & 15) >> 2, p4 = lane & 3, l15 = lane & 15;

  auto issue = [&](int s) {
    char* base = smem + (s % K4_NB) * K4_STG;
#pragma unroll
    for (int i = 0; i < 2; ++i) {
      const int r = 16 * wave + 8 * i + (lane >> 3), c = (lane & 7) ^ (r & 7);
      glds16(hbuf + (size_t)(row0 + r) * F_ + s * 64 + c * 8, base + (16 * wave + 8 * i) * 128);
    }
    char* dst = base + K4_A;
#pragma unroll
    for (int i = 0; i < 3; ++i) {
      const int j = i * 4 + wave;
      if (j < 10) {
        const int ci = j * 64 + lane;
        const int r = ci / 10, c = ci % 10;
        glds16(w2 + (size_t)(s * 64 + r) * 640 + ct * 80 + c * 8, dst + j * 1024);
      }
    }
  };
  STAMP(3, 0);
#pragma unroll
  for (int s = 0; s < K4_NB - 1; ++s) issue(s);
  STAMP(3, 1);
  const int wm = wave & 1, wn = wave >> 1;
  const int u0 = wn * 3;
  u32x2 xr[2][3], bb[3];
#pragma unroll
  for (int u = 0; u < 3; ++u) {
    const int uc = u0 + u < 5 ? u0 + u : 4;
    const int n0 = ct * 80 + uc * 16 + 4 * g;
    bb[u] = *(const u32x2*)(b2 + n0);
#pragma unroll
    for (int i = 0; i < 2; ++i) xr[i][u] = *(const u32x2*)(x1 + (size_t)(row0 + 32 * wm + 16 * i + l15) * 640 + n0);
  }
  f32x4 acc[2][3];
#pragma unroll
  for (int i = 0; i < 2; ++i)
#pragma unroll
    for (int u = 0; u < 3; ++u) acc[i][u] = (f32x4){0.f, 0.f, 0.f, 0.f};
  for (int s = 0; s < K4_NS; ++s) {
    if (s + K4_NB - 2 < K4_NS) { if (wave < 2) wait_vm<20>(); else wait_vm<16>(); } else wait_vm<0>();
    WAIT_LGKM0;
    BAR;
    if (s + K4_NB - 1 < K4_NS) issue(s + K4_NB - 1);
    const char* st = smem + (s % K4_NB) * K4_STG;
#pragma unroll
    for (int kk = 0; kk < 2; ++kk) {
      bf16x8 xf[2];
#pragma unroll
      for (int i = 0; i < 2; ++i)
        xf[i] = *(const bf16x8*)(st + (32 * wm + 16 * i + l15) * 128 + (((4 * kk + g) ^ (l15 & 7)) * 16));
#pragma unroll
      for (int u = 0; u < 3; ++u) {
        const int uc = u0 + u < 5 ? u0 + u : 4;
        const char* b = st + K4_A + (32 * kk + 8 * g + q4) * 160 + uc * 32 + p4 * 8;
        bf16x8 wf = cat8(tr_read(b), tr_read(b + 4 * 160));
#pragma unroll
        for (int i = 0; i < 2; ++i)
          acc[i][u] = __builtin_amdgcn_mfma_f32_16x16x32_bf16(wf, xf[i], acc[i][u], 0, 0, 0);
      }
    }
  }
  STAMP(3, 2);
#pragma unroll
  for (int u = 0; u < 3; ++u) {
    if (u0 + u < 5) {
      const int n0 = ct * 80 + (u0 + u) * 16 + 4 * g;
#pragma unroll
      for (int i = 0; i < 2; ++i) {
        const int m = row0 + 32 * wm + 16 * i + l15;
        float r0 = __uint_as_float(xr[i][u][0] << 16) + bfr(bfr(acc[i][u][0]) + __uint_as_float(bb[u][0] << 16));
        float r1 = __uint_as_float(xr[i][u][0] & 0xFFFF0000u) + bfr(bfr(acc[i][u][1]) + __uint_as_float(bb[u][0] & 0xFFFF0000u));
        float r2 = __uint_as_float(xr[i][u][1] << 16) + bfr(bfr(acc[i][u][2]) + __uint_as_float(bb[u][1] << 16));
        float r3 = __uint_as_float(xr[i][u][1] & 0xFFFF0000u) + bfr(bfr(acc[i][u][3]) + __uint_as_float(bb[u][1] & 0xFFFF0000u));
        u32x2 o = {pack2(r0, r1), pack2(r2, r3)};
        *(u32x2*)(xo + (size_t)m * 640 + n0) = o;
      }
    }
  }
  STAMP(3, 3);
}

extern "C" void kernel_launch(void* const* d_in, const int* in_sizes, int n_in,
                              void* d_out, int out_size, void* d_ws, size_t ws_size,
                              hipStream_t stream) {
  const u16* A_x = (const u16*)d_in[0];
  const u16* wqkv = (const u16*)d_in[1];
  const u16* wo = (const u16*)d_in[2];
  const u16* w1 = (const u16*)d_in[4];
  const u16* b1 = (const u16*)d_in[5];
  const u16* w2 = (const u16*)d_in[6];
  const u16* b2 = (const u16*)d_in[7];
  char* ws = (char*)d_ws;
  unsigned* cnt = (unsigned*)ws;
  u16* pqp = (u16*)(ws + 4096);
  float* part = (float*)(ws + 4096 + 4194304);
  u16* kvf = (u16*)(ws + 4096 + 4194304 + 1720320);
  u16* x1 = (u16*)(ws + 4096 + 4194304 + 1720320 + 131072);
  u16* x2 = x1 + (size_t)S_ * D_;
  u16* hbuf = x2 + (size_t)S_ * D_;
  hipMemsetAsync(cnt, 0, 64, stream);
  const u16* xin = A_x;
  for (int layer = 0; layer < NLAYERS; ++layer) {
    u16* xout = layer == 0 ? x2 : (u16*)d_out;
    k1_qkv<<<256, 512, 0, stream>>>(xin, wqkv, pqp, part, kvf, cnt + 8 * layer);
    k2_attn_out<<<256, 512, 0, stream>>>(xin, wo, pqp, kvf, x1);
    k3_mlp_up<<<256, 512, 0, stream>>>(x1, w1, b1, hbuf);
    k4_mlp_down<<<256, 256, 0, stream>>>(hbuf, w2, b2, x1, xout);
    xin = x2;
  }
}
```

```cpp
#include <hip/hip_runtime.h>
#include <hip/hip_bf16.h>
#include <stdint.h>
#define NLAYERS 2

typedef __attribute__((ext_vector_type(8))) short bf16x8;
typedef __attribute__((ext_vector_type(4))) short s16x4;
typedef __attribute__((ext_vector_type(4))) float f32x4;
typedef __attribute__((ext_vector_type(2))) unsigned u32x2;
typedef __attribute__((ext_vector_type(4))) unsigned u32x4;
typedef unsigned short u16;
#define LDS_AS __attribute__((address_space(3)))

constexpr int S_ = 2048, D_ = 640, F_ = 1728;
constexpr int XS_STRIDE = 1296;
constexpr int XS_BYTES = 64 * XS_STRIDE;

__device__ __forceinline__ float bf2f(u16 v) { return __uint_as_float(((unsigned)v) << 16); }
__device__ __forceinline__ u16 f2bf(float f) {
  unsigned u = __float_as_uint(f);
  u += 0x7FFFu + ((u >> 16) & 1u);
  return (u16)(u >> 16);
}
__device__ __forceinline__ unsigned pack2(float a, float b) {
  unsigned r;
  asm("v_cvt_pk_bf16_f32 %0, %1, %2" : "=v"(r) : "v"(a), "v"(b));
  return r;
}
__device__ __forceinline__ float bfr(float f) { return __uint_as_float(pack2(f, f) & 0xFFFF0000u); }

#define WAIT_VM(n) asm volatile("s_waitcnt vmcnt(" #n ")" ::: "memory")
#define WAIT_LGKM0 asm volatile("s_waitcnt lgkmcnt(0)" ::: "memory")
#define BAR __builtin_amdgcn_s_barrier()
#define STAMP(k, i)

template <int N> __device__ __forceinline__ void wait_vm() {
  if constexpr (N == 0) WAIT_VM(0);
  else if constexpr (N == 2) WAIT_VM(2);
  else if constexpr (N == 3) WAIT_VM(3);
  else if constexpr (N == 4) WAIT_VM(4);
  else if constexpr (N == 6) WAIT_VM(6);
  else if constexpr (N == 8) WAIT_VM(8);
  else if constexpr (N == 9) WAIT_VM(9);
  else if constexpr (N == 12) WAIT_VM(12);
  else if constexpr (N == 14) WAIT_VM(14);
  else if constexpr (N == 15) WAIT_VM(15);
  else if constexpr (N == 18) WAIT_VM(18);
  else if constexpr (N == 20) WAIT_VM(20);
  else if constexpr (N == 24) WAIT_VM(24);
  else if constexpr (N == 1) WAIT_VM(1);
  else if constexpr (N == 13) WAIT_VM(13);
  else if constexpr (N == 16) WAIT_VM(16);
  else if constexpr (N == 10) WAIT_VM(10);
  else WAIT_VM(0);
}

__device__ __forceinline__ void glds16(const void* g, char* lds_base) {
  unsigned keep;
  const unsigned base = __builtin_amdgcn_readfirstlane((unsigned)(size_t)lds_base);
  asm volatile("s_mov_b32 %0, m0\n\ts_mov_b32 m0, %2\n\ts_nop 0\n\tglobal_load_lds_dwordx4 %1, off\n\ts_mov_b32 m0, %0"
               : "=&s"(keep) : "v"(g), "s"(base) : "memory");
}
__device__ __forceinline__ s16x4 tr_read(const char* p) {
  return __builtin_amdgcn_ds_read_tr16_b64_v4i16((LDS_AS s16x4*)p);
}
__device__ __forceinline__ bf16x8 cat8(s16x4 a, s16x4 b) {
  bf16x8 r;
  r[0] = a[0]; r[1] = a[1]; r[2] = a[2]; r[3] = a[3];
  r[4] = b[0]; r[5] = b[1]; r[6] = b[2]; r[7] = b[3];
  return r;
}

__device__ __forceinline__ void ln_tile(const u16* __restrict__ x, int row0, char* xs, int wave, int lane) {
  const int j = lane & 15, rsub = lane >> 4;
  u32x4 q[2][5];
#pragma unroll
  for (int pass = 0; pass < 2; ++pass) {
    const u16* src = x + (size_t)(row0 + wave * 8 + pass * 4 + rsub) * D_;
#pragma unroll
    for (int c = 0; c < 5; ++c) q[pass][c] = *(const u32x4*)(src + (j + 16 * c) * 8);
  }
#pragma unroll
  for (int pass = 0; pass < 2; ++pass) {
    const int r = wave * 8 + pass * 4 + rsub;
    float v[40];
#pragma unroll
    for (int c = 0; c < 5; ++c) {
#pragma unroll
      for (int e = 0; e < 4; ++e) {
        v[c * 8 + 2 * e] = __uint_as_float(q[pass][c][e] << 16);
        v[c * 8 + 2 * e + 1] = __uint_as_float(q[pass][c][e] & 0xFFFF0000u);
      }
    }
    float s = 0.f;
#pragma unroll
    for (int e = 0; e < 40; ++e) s += v[e];
    s += __shfl_xor(s, 1); s += __shfl_xor(s, 2); s += __shfl_xor(s, 4); s += __shfl_xor(s, 8);
    const float mean = s * (1.f / 640.f);
    float q2 = 0.f;
#pragma unroll
    for (int e = 0; e < 40; ++e) { float d = v[e] - mean; q2 += d * d; }
    q2 += __shfl_xor(q2, 1); q2 += __shfl_xor(q2, 2); q2 += __shfl_xor(q2, 4); q2 += __shfl_xor(q2, 8);
    const float rstd = __frsqrt_rn(q2 * (1.f / 640.f) + 1e-5f);
    const float nmr = -mean * rstd;
#pragma unroll
    for (int c = 0; c < 5; ++c) {
      u32x4 o;
#pragma unroll
      for (int e = 0; e < 4; ++e)
        o[e] = pack2(fmaf(v[c * 8 + 2 * e], rstd, nmr), fmaf(v[c * 8 + 2 * e + 1], rstd, nmr));
      *(u32x4*)(xs + r * XS_STRIDE + (j + 16 * c) * 16) = o;
    }
  }
}

constexpr int K1_STG = 32 * 480;
constexpr int K1_NB = 5;
__global__ __launch_bounds__(512, 1) void k1_qkv(const u16* __restrict__ x, const u16* __restrict__ wqkv,
                                                 u16* __restrict__ pqp, float* __restrict__ part,
                                                 u16* __restrict__ kvf, unsigned* __restrict__ cnt) {
  __shared__ __attribute__((aligned(16))) char smem[XS_BYTES + K1_NB * K1_STG];
  char* xs = smem;
  char* ring = smem + XS_BYTES;
  const int tid = threadIdx.x, lane = tid & 63, wave = __builtin_amdgcn_readfirstlane(tid >> 6);
  const int hg = (blockIdx.x >> 3) & 7, rt = (blockIdx.x & 7) * 4 + (blockIdx.x >> 6);
  const int row0 = rt * 64;
  const int g = lane >> 4, q4 = (lane & 15) >> 2, p4 = lane & 3, l15 = lane & 15;

  auto issue = [&](int s) {
    char* dst = ring + (s % K1_NB) * K1_STG;
#pragma unroll
    for (int i = 0; i < 2; ++i) {
      const int j = i * 8 + wave;
      if (j < 15) {
        const int ci = j * 64 + lane;
        const int r = ci / 30, c = ci % 30;
        const int col = (c / 10) * 640 + hg * 80 + (c % 10) * 8;
        glds16(wqkv + (size_t)(s * 32 + r) * 1920 + col, dst + j * 1024);
      }
    }
  };
  STAMP(0, 0);
  issue(0); issue(1); issue(2); issue(3);
  ln_tile(x, row0, xs, wave, lane);
  STAMP(0, 1);

  const int wm = wave & 1, wn = wave >> 1;
  const int u0 = wn * 4;
  f32x4 acc[2][4];
#pragma unroll
  for (int i = 0; i < 2; ++i)
#pragma unroll
    for (int u = 0; u < 4; ++u) acc[i][u] = (f32x4){0.f, 0.f, 0.f, 0.f};

  auto rd = [&](int s, bf16x8 (&xf)[2], bf16x8 (&wf)[4]) {
    const char* st = ring + (s % K1_NB) * K1_STG;
#pragma unroll
    for (int i = 0; i < 2; ++i)
      xf[i] = *(const bf16x8*)(xs + (32 * wm + 16 * i + l15) * XS_STRIDE + (32 * s + 8 * g) * 2);
#pragma unroll
    for (int u = 0; u < 4; ++u) {
      const int uc = u0 + u < 15 ? u0 + u : 14;
      const char* b = st + (8 * g + q4) * 480 + uc * 32 + p4 * 8;
      wf[u] = cat8(tr_read(b), tr_read(b + 4 * 480));
    }
  };
  auto mm = [&](bf16x8 (&xf)[2], bf16x8 (&wf)[4]) {
#pragma unroll
    for (int u = 0; u < 4; ++u)
#pragma unroll
      for (int i = 0; i < 2; ++i)
        acc[i][u] = __builtin_amdgcn_mfma_f32_16x16x32_bf16(wf[u], xf[i], acc[i][u], 0, 0, 0);
  };
  auto sync = [&](int s) {
    if (s + K1_NB - 1 < 20) { if (wave < 7) wait_vm<6>(); else wait_vm<3>(); } else wait_vm<0>();
    WAIT_LGKM0;
    BAR;
    if (s >= 0 && s + K1_NB < 20) issue(s + K1_NB);
  };
  issue(4);
  bf16x8 xa[2], wa[4], xb[2], wb[4];
  sync(-1);
  rd(0, xa, wa);
  for (int s = 0; s < 20; s += 2) {
    sync(s);
    rd(s + 1, xb, wb);
    mm(xa, wa);
    sync(s + 1);
    if (s + 2 < 20) rd(s + 2, xa, wa);
    mm(xb, wb);
  }
  WAIT_LGKM0;
  BAR;
  STAMP(0, 2);

  char* pkimg = ring;
  char* vimg = ring + 64 * 192;
#pragma unroll
  for (int u = 0; u < 4; ++u) {
    const int uu = u0 + u;
    if (uu < 15) {
      const int seg = uu / 5;
      const int n0 = (uu % 5) * 16 + 4 * g;
#pragma unroll
      for (int i = 0; i < 2; ++i) {
        const int m = 32 * wm + 16 * i + l15;
        float vals[4];
#pragma unroll
        for (int r = 0; r < 4; ++r) {
          float t = bfr(acc[i][u][r]);
          if (seg < 2) { t = (t > 0.f ? t + 1.f : __expf(t)) + 1e-6f; }
          vals[r] = t;
        }
        u32x2 pk = {pack2(vals[0], vals[1]), pack2(vals[2], vals[3])};
        const int hh = n0 / 20, d0 = n0 % 20;
        if (seg == 0) {
          *(u32x2*)(pqp + ((size_t)(row0 + m) * 32 + hg * 4 + hh) * 32 + d0) = pk;
        } else if (seg == 1) {
          *(u32x2*)(pkimg + m * 192 + n0 * 2) = pk;
        } else {
          *(u32x2*)(vimg + m * 272 + (hh * 32 + d0) * 2) = pk;
        }
      }
    }
  }
  if (tid < 256) {
    const int r = tid >> 2, hh = tid & 3;
    u32x2 z2 = {0u, 0u};
    u16* dst = pqp + ((size_t)(row0 + r) * 32 + hg * 4 + hh) * 32 + 20;
    *(u32x2*)(dst) = z2; *(u32x2*)(dst + 4) = z2; *(u32x2*)(dst + 8) = z2;
    u32x2 one = {0x3F80u, 0u};
    *(u32x2*)(vimg + r * 272 + (hh * 32 + 20) * 2) = one;
  }
  __syncthreads();
  if (wave < 4) {
    const int hh = wave;
    f32x4 kv[2][2];
#pragma unroll
    for (int a = 0; a < 2; ++a)
#pragma unroll
      for (int b = 0; b < 2; ++b) kv[a][b] = (f32x4){0.f, 0.f, 0.f, 0.f};
#pragma unroll
    for (int ks = 0; ks < 2; ++ks) {
      bf16x8 af[2], bfv[2];
#pragma unroll
      for (int dt = 0; dt < 2; ++dt) {
        const char* a = pkimg + (32 * ks + 8 * g + q4) * 192 + (hh * 20 + 16 * dt + 4 * p4) * 2;
        af[dt] = cat8(tr_read(a), tr_read(a + 4 * 192));
      }
#pragma unroll
      for (int et = 0; et < 2; ++et) {
        const char* b = vimg + (32 * ks + 8 * g + q4) * 272 + (hh * 32 + 16 * et + 4 * p4) * 2;
        bfv[et] = cat8(tr_read(b), tr_read(b + 4 * 272));
      }
#pragma unroll
      for (int dt = 0; dt < 2; ++dt)
#pragma unroll
        for (int et = 0; et < 2; ++et)
          kv[dt][et] = __builtin_amdgcn_mfma_f32_16x16x32_bf16(af[dt], bfv[et], kv[dt][et], 0, 0, 0);
    }
    float* pp = part + ((size_t)(hg * 32 + rt) * 4 + hh) * 420;
#pragma unroll
    for (int dt = 0; dt < 2; ++dt)
#pragma unroll
      for (int et = 0; et < 2; ++et) {
        const int e = 16 * et + l15, d0 = 16 * dt + 4 * g;
        if (e < 21 && d0 < 20) {
#pragma unroll
          for (int r = 0; r < 4; ++r)
            __hip_atomic_store(pp + e * 20 + d0 + r, kv[dt][et][r], __ATOMIC_RELAXED, __HIP_MEMORY_SCOPE_AGENT);
        }
      }
  }
  STAMP(0, 3);
  asm volatile("s_waitcnt vmcnt(0)" ::: "memory");
  __syncthreads();
  unsigned* flag = (unsigned*)xs;
  if (tid == 0) {
    unsigned old = __hip_atomic_fetch_add(cnt + hg, 1u, __ATOMIC_RELAXED, __HIP_MEMORY_SCOPE_AGENT);
    unsigned last = (old == 31u) ? 1u : 0u;
    if (last) {
      __builtin_amdgcn_fence(__ATOMIC_ACQUIRE, "agent");
      asm volatile("s_waitcnt vmcnt(0)" ::: "memory");
    }
    *flag = last;
  }
  __syncthreads();
  if (*flag) {
    const f32x4* pb = (const f32x4*)(part + (size_t)(hg * 32) * 4 * 420);
    f32x4* red = (f32x4*)(smem + 1024);
    {
      f32x4 v[7][4];
#pragma unroll
      for (int k = 0; k < 7; ++k) {
        int task = k * 512 + tid;
        task = task < 3360 ? task : 3359;
        const int rg = task / 420, f = task % 420;
#pragma unroll
        for (int r = 0; r < 4; ++r) v[k][r] = pb[(size_t)(rg * 4 + r) * 420 + f];
      }
#pragma unroll
      for (int k = 0; k < 7; ++k) {
        const int task = k * 512 + tid;
        if (task < 3360) red[task] = (v[k][0] + v[k][1]) + (v[k][2] + v[k][3]);
      }
    }
    __syncthreads();
    const float* redf = (const float*)red;
    for (int o = tid; o < 4 * 1536; o += 512) {
      const int hh = o / 1536, er = (o % 1536) >> 5, d = o & 31;
      const int e = er >= 32 ? 20 : er;
      float sum = 0.f;
      if (e < 21 && d < 20) {
        const int idx = hh * 420 + e * 20 + d;
#pragma unroll
        for (int rg = 0; rg < 8; ++rg) sum += redf[rg * 1680 + idx];
      }
      kvf[(size_t)(hg * 4 + hh) * 1536 + er * 32 + d] = f2bf(sum);
    }
  }
  STAMP(0, 4);
}

constexpr int K2_STG = 32 * 160;
constexpr int K2_NB = 15;
__global__ __launch_bounds__(512, 1) void k2_attn_out(const u16* __restrict__ x, const u16* __restrict__ wo,
                                                      const u16* __restrict__ pqp, const u16* __restrict__ kvf,
                                                      u16* __restrict__ x1) {
  __shared__ __attribute__((aligned(16))) char smem[XS_BYTES + K2_NB * K2_STG];
  char* xs = smem;
  char* ring = smem + XS_BYTES;
  const int tid = threadIdx.x, lane = tid & 63, wave = __builtin_amdgcn_readfirstlane(tid >> 6);
  const int ct = (blockIdx.x >> 3) & 7, rt = (blockIdx.x & 7) * 4 + (blockIdx.x >> 6);
  const int row0 = rt * 64;
  const int g = lane >> 4, q4 = (lane & 15) >> 2, p4 = lane & 3, l15 = lane & 15;

  auto issue = [&](int s) {
    char* dst = ring + (s % K2_NB) * K2_STG;
    {
      const int j = wave;
      if (j < 5) {
        const int ci = j * 64 + lane;
        const int r = ci / 10, c = ci % 10;
        glds16(wo + (size_t)(s * 32 + r) * 640 + ct * 80 + c * 8, dst + j * 1024);
      }
    }
  };
  STAMP(1, 0);
#pragma unroll
  for (int s = 0; s < K2_NB - 1; ++s) issue(s);

  {
    bf16x8 kf[4][3], pf[4][4];
#pragma unroll
    for (int hi = 0; hi < 4; ++hi) {
      const int h = wave * 4 + hi;
#pragma unroll
      for (int et = 0; et < 3; ++et) kf[hi][et] = *(const bf16x8*)(kvf + (size_t)h * 1536 + (16 * et + l15) * 32 + 8 * g);
#pragma unroll
      for (int i = 0; i < 4; ++i) pf[hi][i] = *(const bf16x8*)(pqp + ((size_t)(row0 + 16 * i + l15) * 32 + h) * 32 + 8 * g);
    }
#pragma unroll
    for (int hi = 0; hi < 4; ++hi) {
      const int h = wave * 4 + hi;
#pragma unroll
      for (int i = 0; i < 4; ++i) {
        const int m = 16 * i + l15;
        const f32x4 zz = {0.f, 0.f, 0.f, 0.f};
        f32x4 a0 = __builtin_amdgcn_mfma_f32_16x16x32_bf16(kf[hi][0], pf[hi][i], zz, 0, 0, 0);
        f32x4 a1 = __builtin_amdgcn_mfma_f32_16x16x32_bf16(kf[hi][1], pf[hi][i], zz, 0, 0, 0);
        f32x4 ad = __builtin_amdgcn_mfma_f32_16x16x32_bf16(kf[hi][2], pf[hi][i], zz, 0, 0, 0);
        const float inv = __frcp_rn(ad[0] + 1e-6f);
        u32x2 o0 = {pack2(a0[0] * inv, a0[1] * inv), pack2(a0[2] * inv, a0[3] * inv)};
        *(u32x2*)(xs + m * XS_STRIDE + (h * 20 + 4 * g) * 2) = o0;
        if (g == 0) {
          u32x2 o1 = {pack2(a1[0] * inv, a1[1] * inv), pack2(a1[2] * inv, a1[3] * inv)};
          *(u32x2*)(xs + m * XS_STRIDE + (h * 20 + 16) * 2) = o1;
        }
      }
    }
  }

  STAMP(1, 1);
  const int wm = wave & 3, wn = wave >> 2;
  const int u0 = wn * 3;
  const int m = row0 + 16 * wm + l15;
  u32x2 xr[3];
#pragma unroll
  for (int u = 0; u < 3; ++u) {
    const int uu = u0 + u < 5 ? u0 + u : 4;
    xr[u] = *(const u32x2*)(x + (size_t)m * 640 + ct * 80 + uu * 16 + 4 * g);
  }
  f32x4 acc[3];
#pragma unroll
  for (int u = 0; u < 3; ++u) acc[u] = (f32x4){0.f, 0.f, 0.f, 0.f};
  auto rd = [&](int s, bf16x8& xf, bf16x8 (&wf)[3]) {
    const char* st = ring + (s % K2_NB) * K2_STG;
    xf = *(const bf16x8*)(xs + (16 * wm + l15) * XS_STRIDE + (32 * s + 8 * g) * 2);
#pragma unroll
    for (int u = 0; u < 3; ++u) {
      const int uc = u0 + u < 5 ? u0 + u : 4;
      const char* b = st + (8 * g + q4) * 160 + uc * 32 + p4 * 8;
      wf[u] = cat8(tr_read(b), tr_read(b + 4 * 160));
    }
  };
  auto mm = [&](bf16x8& xf, bf16x8 (&wf)[3]) {
#pragma unroll
    for (int u = 0; u < 3; ++u) acc[u] = __builtin_amdgcn_mfma_f32_16x16x32_bf16(wf[u], xf, acc[u], 0, 0, 0);
  };
  auto sync = [&](int s) {
    if (s + K2_NB - 1 < 20) { if (wave < 5) wait_vm<13>(); } else wait_vm<0>();
    WAIT_LGKM0;
    BAR;
    if (s >= 0 && s + K2_NB < 20) issue(s + K2_NB);
  };
  issue(K2_NB - 1);
  bf16x8 xa, wa[3], xb, wb[3];
  sync(-1);
  rd(0, xa, wa);
  for (int s = 0; s < 20; s += 2) {
    sync(s);
    rd(s + 1, xb, wb);
    mm(xa, wa);
    sync(s + 1);
    if (s + 2 < 20) rd(s + 2, xa, wa);
    mm(xb, wb);
  }
  STAMP(1, 2);
#pragma unroll
  for (int u = 0; u < 3; ++u) {
    if (u0 + u < 5) {
      const int n0 = ct * 80 + (u0 + u) * 16 + 4 * g;
      float r0 = __uint_as_float(xr[u][0] << 16) + bfr(acc[u][0]);
      float r1 = __uint_as_float(xr[u][0] & 0xFFFF0000u) + bfr(acc[u][1]);
      float r2 = __uint_as_float(xr[u][1] << 16) + bfr(acc[u][2]);
      float r3 = __uint_as_float(xr[u][1] & 0xFFFF0000u) + bfr(acc[u][3]);
      u32x2 o = {pack2(r0, r1), pack2(r2, r3)};
      *(u32x2*)(x1 + (size_t)m * 640 + n0) = o;
    }
  }
  STAMP(1, 3);
}

constexpr int K3_STG = 32 * 448;
constexpr int K3_NB = 5;
__global__ __launch_bounds__(512, 1) void k3_mlp_up(const u16* __restrict__ x1, const u16* __restrict__ w1,
                                                    const u16* __restrict__ b1, u16* __restrict__ hbuf) {
  __shared__ __attribute__((aligned(16))) char smem[XS_BYTES + K3_NB * K3_STG];
  char* xs = smem;
  char* ring = smem + XS_BYTES;
  const int tid = threadIdx.x, lane = tid & 63, wave = __builtin_amdgcn_readfirstlane(tid >> 6);
  const int ct = (blockIdx.x >> 3) & 7, rt = (blockIdx.x & 7) * 4 + (blockIdx.x >> 6);
  const int row0 = rt * 64;
  const int g = lane >> 4, q4 = (lane & 15) >> 2, p4 = lane & 3, l15 = lane & 15;

  auto issue = [&](int s) {
    char* dst = ring + (s % K3_NB) * K3_STG;
#pragma unroll
    for (int i = 0; i < 2; ++i) {
      const int j = i * 8 + wave;
      if (j < 14) {
        const int ci = j * 64 + lane;
        const int r = ci / 28, c = ci % 28;
        int col = ct * 224 + c * 8;
        col = col < F_ - 8 ? col : F_ - 8;
        glds16(w1 + (size_t)(s * 32 + r) * F_ + col, dst + j * 1024);
      }
    }
  };
  STAMP(2, 0);
  issue(0); issue(1); issue(2); issue(3);
  ln_tile(x1, row0, xs, wave, lane);
  STAMP(2, 1);

  const int wm = wave & 1, wn = wave >> 1;
  const int u0 = wn * 4;
  f32x4 acc[2][4];
#pragma unroll
  for (int i = 0; i < 2; ++i)
#pragma unroll
    for (int u = 0; u < 4; ++u) acc[i][u] = (f32x4){0.f, 0.f, 0.f, 0.f};
  u32x2 bb[4];
#pragma unroll
  for (int u = 0; u < 4; ++u) {
    int n0 = ct * 224 + (u0 + u) * 16 + 4 * g;
    n0 = (u0 + u < 14 && n0 < F_) ? n0 : 0;
    bb[u] = *(const u32x2*)(b1 + n0);
  }

  auto rd = [&](int s, bf16x8 (&xf)[2], bf16x8 (&wf)[4]) {
    const char* st = ring + (s % K3_NB) * K3_STG;
#pragma unroll
    for (int i = 0; i < 2; ++i)
      xf[i] = *(const bf16x8*)(xs + (32 * wm + 16 * i + l15) * XS_STRIDE + (32 * s + 8 * g) * 2);
#pragma unroll
    for (int u = 0; u < 4; ++u) {
      const int uc = u0 + u < 14 ? u0 + u : 13;
      const char* b = st + (8 * g + q4) * 448 + uc * 32 + p4 * 8;
      wf[u] = cat8(tr_read(b), tr_read(b + 4 * 448));
    }
  };
  auto mm = [&](bf16x8 (&xf)[2], bf16x8 (&wf)[4]) {
#pragma unroll
    for (int u = 0; u < 4; ++u)
#pragma unroll
      for (int i = 0; i < 2; ++i)
        acc[i][u] = __builtin_amdgcn_mfma_f32_16x16x32_bf16(wf[u], xf[i], acc[i][u], 0, 0, 0);
  };
  auto sync = [&](int s) {
    if (s + K3_NB - 1 < 20) { if (wave < 6) wait_vm<6>(); else wait_vm<3>(); } else wait_vm<0>();
    WAIT_LGKM0;
    BAR;
    if (s >= 0 && s + K3_NB < 20) issue(s + K3_NB);
  };
  issue(4);
  bf16x8 xa[2], wa[4], xb[2], wb[4];
  sync(-1);
  rd(0, xa, wa);
  for (int s = 0; s < 20; s += 2) {
    sync(s);
    rd(s + 1, xb, wb);
    mm(xa, wa);
    sync(s + 1);
    if (s + 2 < 20) rd(s + 2, xa, wa);
    mm(xb, wb);
  }
  STAMP(2, 2);
#pragma unroll
  for (int u = 0; u < 4; ++u) {
    const int n0 = ct * 224 + (u0 + u) * 16 + 4 * g;
    if (u0 + u < 14 && n0 < F_) {
      float bv[4] = {__uint_as_float(bb[u][0] << 16), __uint_as_float(bb[u][0] & 0xFFFF0000u),
                     __uint_as_float(bb[u][1] << 16), __uint_as_float(bb[u][1] & 0xFFFF0000u)};
#pragma unroll
      for (int i = 0; i < 2; ++i) {
        const int m = row0 + 32 * wm + 16 * i + l15;
        float o[4];
#pragma unroll
        for (int r = 0; r < 4; ++r) {
          float t = bfr(acc[i][u][r] + bv[r]);
          float inner = 0.7978845608028654f * (t + 0.044715f * t * t * t);
          inner = fminf(fmaxf(inner, -15.f), 15.f);
          float e2 = __expf(2.f * inner);
          float th = 1.f - 2.f * __frcp_rn(e2 + 1.f);
          o[r] = 0.5f * t * (1.0f + th);
        }
        u32x2 ov = {pack2(o[0], o[1]), pack2(o[2], o[3])};
        *(u32x2*)(hbuf + (size_t)m * F_ + n0) = ov;
      }
    }
  }
  STAMP(2, 3);
}

constexpr int K4_A = 64 * 128;
constexpr int K4_B = 64 * 160;
constexpr int K4_STG = K4_A + K4_B;
constexpr int K4_NB = 8;
constexpr int K4_NS = 27;
__global__ __launch_bounds__(256, 1) void k4_mlp_down(const u16* __restrict__ hbuf, const u16* __restrict__ w2,
                                                      const u16* __restrict__ b2, const u16* __restrict__ x1,
                                                      u16* __restrict__ xo) {
  __shared__ __attribute__((aligned(16))) char smem[K4_NB * K4_STG];
  const int tid = threadIdx.x, lane = tid & 63, wave = __builtin_amdgcn_readfirstlane(tid >> 6);
  const int ct = (blockIdx.x >> 3) & 7, rt = (blockIdx.x & 7) * 4 + (blockIdx.x >> 6);
  const int row0 = rt * 64;
  const int g = lane >> 4, q4 = (lane & 15) >> 2, p4 = lane & 3, l15 = lane & 15;

  auto issue = [&](int s) {
    char* base = smem + (s % K4_NB) * K4_STG;
#pragma unroll
    for (int i = 0; i < 2; ++i) {
      const int r = 16 * wave + 8 * i + (lane >> 3), c = (lane & 7) ^ (r & 7);
      glds16(hbuf + (size_t)(row0 + r) * F_ + s * 64 + c * 8, base + (16 * wave + 8 * i) * 128);
    }
    char* dst = base + K4_A;
#pragma unroll
    for (int i = 0; i < 3; ++i) {
      const int j = i * 4 + wave;
      if (j < 10) {
        const int ci = j * 64 + lane;
        const int r = ci / 10, c = ci % 10;
        glds16(w2 + (size_t)(s * 64 + r) * 640 + ct * 80 + c * 8, dst + j * 1024);
      }
    }
  };
  STAMP(3, 0);
#pragma unroll
  for (int s = 0; s < K4_NB - 2; ++s) issue(s);
  STAMP(3, 1);
  const int wm = wave & 1, wn = wave >> 1;
  const int u0 = wn * 3;
  u32x2 xr[2][3], bb[3];
#pragma unroll
  for (int u = 0; u < 3; ++u) {
    const int uc = u0 + u < 5 ? u0 + u : 4;
    const int n0 = ct * 80 + uc * 16 + 4 * g;
    bb[u] = *(const u32x2*)(b2 + n0);
#pragma unroll
    for (int i = 0; i < 2; ++i) xr[i][u] = *(const u32x2*)(x1 + (size_t)(row0 + 32 * wm + 16 * i + l15) * 640 + n0);
  }
  f32x4 acc[2][3];
#pragma unroll
  for (int i = 0; i < 2; ++i)
#pragma unroll
    for (int u = 0; u < 3; ++u) acc[i][u] = (f32x4){0.f, 0.f, 0.f, 0.f};
  for (int s0 = 0; s0 < K4_NS; s0 += 2) {
    if (s0 + 6 < K4_NS) { if (wave < 2) wait_vm<20>(); else wait_vm<16>(); } else wait_vm<0>();
    WAIT_LGKM0;
    BAR;
    if (s0 + 6 < K4_NS) issue(s0 + 6);
    if (s0 + 7 < K4_NS) issue(s0 + 7);
#pragma unroll
    for (int ss = 0; ss < 2; ++ss) {
      const int s = s0 + ss;
      if (s < K4_NS) {
        const char* st = smem + (s % K4_NB) * K4_STG;
#pragma unroll
        for (int kk = 0; kk < 2; ++kk) {
          bf16x8 xf[2];
#pragma unroll
          for (int i = 0; i < 2; ++i)
            xf[i] = *(const bf16x8*)(st + (32 * wm + 16 * i + l15) * 128 + (((4 * kk + g) ^ (l15 & 7)) * 16));
#pragma unroll
          for (int u = 0; u < 3; ++u) {
            const int uc = u0 + u < 5 ? u0 + u : 4;
            const char* b = st + K4_A + (32 * kk + 8 * g + q4) * 160 + uc * 32 + p4 * 8;
            bf16x8 wf = cat8(tr_read(b), tr_read(b + 4 * 160));
#pragma unroll
            for (int i = 0; i < 2; ++i)
              acc[i][u] = __builtin_amdgcn_mfma_f32_16x16x32_bf16(wf, xf[i], acc[i][u], 0, 0, 0);
          }
        }
      }
    }
  }
  STAMP(3, 2);
#pragma unroll
  for (int u = 0; u < 3; ++u) {
    if (u0 + u < 5) {
      const int n0 = ct * 80 + (u0 + u) * 16 + 4 * g;
#pragma unroll
      for (int i = 0; i < 2; ++i) {
        const int m = row0 + 32 * wm + 16 * i + l15;
        float r0 = __uint_as_float(xr[i][u][0] << 16) + bfr(bfr(acc[i][u][0]) + __uint_as_float(bb[u][0] << 16));
        float r1 = __uint_as_float(xr[i][u][0] & 0xFFFF0000u) + bfr(bfr(acc[i][u][1]) + __uint_as_float(bb[u][0] & 0xFFFF0000u));
        float r2 = __uint_as_float(xr[i][u][1] << 16) + bfr(bfr(acc[i][u][2]) + __uint_as_float(bb[u][1] << 16));
        float r3 = __uint_as_float(xr[i][u][1] & 0xFFFF0000u) + bfr(bfr(acc[i][u][3]) + __uint_as_float(bb[u][1] & 0xFFFF0000u));
        u32x2 o = {pack2(r0, r1), pack2(r2, r3)};
        *(u32x2*)(xo + (size_t)m * 640 + n0) = o;
      }
    }
  }
  STAMP(3, 3);
}

extern "C" void kernel_launch(void* const* d_in, const int* in_sizes, int n_in,
                              void* d_out, int out_size, void* d_ws, size_t ws_size,
                              hipStream_t stream) {
  const u16* A_x = (const u16*)d_in[0];
  const u16* wqkv = (const u16*)d_in[1];
  const u16* wo = (const u16*)d_in[2];
  const u16* w1 = (const u16*)d_in[4];
  const u16* b1 = (const u16*)d_in[5];
  const u16* w2 = (const u16*)d_in[6];
  const u16* b2 = (const u16*)d_in[7];
  char* ws = (char*)d_ws;
  unsigned* cnt = (unsigned*)ws;
  u16* pqp = (u16*)(ws + 4096);
  float* part = (float*)(ws + 4096 + 4194304);
  u16* kvf = (u16*)(ws + 4096 + 4194304 + 1720320);
  u16* x1 = (u16*)(ws + 4096 + 4194304 + 1720320 + 131072);
  u16* x2 = x1 + (size_t)S_ * D_;
  u16* hbuf = x2 + (size_t)S_ * D_;
  hipMemsetAsync(cnt, 0, 64, stream);
  const u16* xin = A_x;
  for (int layer = 0; layer < NLAYERS; ++layer) {
    u16* xout = layer == 0 ? x2 : (u16*)d_out;
    k1_qkv<<<256, 512, 0, stream>>>(xin, wqkv, pqp, part, kvf, cnt + 8 * layer);
    k2_attn_out<<<256, 512, 0, stream>>>(xin, wo, pqp, kvf, x1);
    k3_mlp_up<<<256, 512, 0, stream>>>(x1, w1, b1, hbuf);
    k4_mlp_down<<<256, 256, 0, stream>>>(hbuf, w2, b2, x1, xout);
    xin = x2;
  }
}
```
